```python
import torch

def _softmax(x, axis=-1):
    return torch.softmax(x, dim=axis)
def _gelu(x):
    return torch.nn.functional.gelu(x, approximate="tanh")
def _silu(x):
    return torch.nn.functional.silu(x)
def _sigmoid(x):
    return torch.sigmoid(x)
def _layernorm(x, axis=-1, eps=1e-5):
    m = x.mean(dim=axis, keepdim=True)
    v = x.var(dim=axis, keepdim=True, unbiased=False)
    return (x - m) / torch.sqrt(v + eps)
def _scatter_add(tgt, idx, src, axis=0):
    return tgt.clone().index_add_(axis, idx, src)
def _segment_sum(src, seg, n):
    out = torch.zeros((n,) + src.shape[1:], dtype=src.dtype)
    return out.index_add_(0, seg, src)
def _pad(x, pw):
    flat = tuple(v for pair in reversed(pw) for v in pair)
    return torch.nn.functional.pad(x, flat)

kernel_name = 'standalone__mha_softmax__near_ridge'

def setup_inputs():

    _w = (lambda a: (lambda t: t.to(torch.bfloat16) if t.is_floating_point() else t)(torch.as_tensor(a)))
    B, H, Sq, Sk, D = 8, 16, 384, 1024, 256
    return {
        "q": _w((lambda *s: torch.randn(*s, dtype=torch.bfloat16) * 0.8)(B, H, Sq, D)),
        "k": _w((lambda *s: torch.randn(*s, dtype=torch.bfloat16) * 0.8)(B, H, Sk, D)),
        "v": _w((lambda *s: torch.randn(*s, dtype=torch.bfloat16) * 0.05)(B, H, Sk, D)),
    }

def reference(q, k, v):
    scale = q.shape[-1] ** -0.5
    scores = torch.einsum("bhqd,bhkd->bhqk", q, k) * scale
    p = _softmax(scores, axis=-1)
    return torch.einsum("bhqk,bhkd->bhqd", p, v)
```

```cpp
#include <hip/hip_runtime.h>
#include <hip/hip_bf16.h>


__global__ __launch_bounds__(256, 1) void mha_fwd(const __hip_bfloat16* __restrict__ Q,
                                                  const __hip_bfloat16* __restrict__ K,
                                                  const __hip_bfloat16* __restrict__ V,
                                                  __hip_bfloat16* __restrict__ O) {
  __shared__ __attribute__((aligned(16))) char lds[131072];
  const int wid = __builtin_amdgcn_readfirstlane(threadIdx.x >> 6);
  const int bid = blockIdx.x;
  const int head = (bid & 7) + 8 * (bid >> 4);
  const int qhalf = (bid >> 3) & 1;
  const char* qp = (const char*)(Q + ((size_t)head * 384 + qhalf * 192 + wid * 48) * 256);
  const char* kp = (const char*)(K + (size_t)head * 1024 * 256);
  const char* vp = (const char*)(V + (size_t)head * 1024 * 256);
  char* op = (char*)(O + ((size_t)head * 384 + qhalf * 192 + wid * 48) * 256);
  unsigned w8 = wid * 8192u + (unsigned)(size_t)lds;
  asm volatile(
      "s_mov_b64 s[88:89], %1\n"
      "s_mov_b64 s[90:91], %2\n"
      "v_mbcnt_lo_u32_b32 v0, -1, 0\n"
      "v_mbcnt_hi_u32_b32 v0, -1, v0\n"
      "v_and_b32 v1, 15, v0\n"
      "v_lshrrev_b32 v2, 4, v0\n"
      "v_lshrrev_b32 v3, 5, v0\n"
      "v_and_b32 v4, 31, v0\n"
      "v_xor_b32 v5, v4, v3\n"
      "v_lshlrev_b32 v5, 4, v5\n"
      "v_lshl_add_u32 v11, v3, 9, v5\n"
      "v_add_u32 v11, %4, v11\n"
      "v_lshlrev_b32 v5, 1, v3\n"
      "v_xor_b32 v5, v4, v5\n"
      "v_lshlrev_b32 v5, 4, v5\n"
      "v_lshl_add_u32 v12, v3, 9, v5\n"
      "v_add_u32 v12, %4, v12\n"
      "v_lshlrev_b32 v3, 9, v1\n"
      "v_lshl_add_u32 v3, v2, 4, v3\n"
      "v_add_u32 v7, 0, v3\n"
      "global_load_dwordx4 v[106:109], v7, %0 offset:0\n"
      "global_load_dwordx4 v[110:113], v7, %0 offset:64\n"
      "global_load_dwordx4 v[114:117], v7, %0 offset:128\n"
      "global_load_dwordx4 v[118:121], v7, %0 offset:192\n"
      "global_load_dwordx4 v[122:125], v7, %0 offset:256\n"
      "global_load_dwordx4 v[126:129], v7, %0 offset:320\n"
      "global_load_dwordx4 v[130:133], v7, %0 offset:384\n"
      "global_load_dwordx4 v[134:137], v7, %0 offset:448\n"
      "v_add_u32 v8, 8192, v3\n"
      "global_load_dwordx4 v[138:141], v8, %0 offset:0\n"
      "global_load_dwordx4 v[142:145], v8, %0 offset:64\n"
      "global_load_dwordx4 v[146:149], v8, %0 offset:128\n"
      "global_load_dwordx4 v[150:153], v8, %0 offset:192\n"
      "global_load_dwordx4 v[154:157], v8, %0 offset:256\n"
      "global_load_dwordx4 v[158:161], v8, %0 offset:320\n"
      "global_load_dwordx4 v[162:165], v8, %0 offset:384\n"
      "global_load_dwordx4 v[166:169], v8, %0 offset:448\n"
      "v_add_u32 v9, 16384, v3\n"
      "global_load_dwordx4 v[170:173], v9, %0 offset:0\n"
      "global_load_dwordx4 v[174:177], v9, %0 offset:64\n"
      "global_load_dwordx4 v[178:181], v9, %0 offset:128\n"
      "global_load_dwordx4 v[182:185], v9, %0 offset:192\n"
      "global_load_dwordx4 v[186:189], v9, %0 offset:256\n"
      "global_load_dwordx4 v[190:193], v9, %0 offset:320\n"
      "global_load_dwordx4 v[194:197], v9, %0 offset:384\n"
      "global_load_dwordx4 v[198:201], v9, %0 offset:448\n"
      "s_add_u32 m0, %4, 0\n"
      "v_xor_b32 v10, 0, v11\n"
      "global_load_lds_dwordx4 v10, s[88:89]\n"
      "s_add_u32 m0, m0, 1024\n"
      "v_xor_b32 v10, 1056, v11\n"
      "global_load_lds_dwordx4 v10, s[88:89]\n"
      "s_add_u32 m0, m0, 1024\n"
      "v_xor_b32 v10, 2112, v11\n"
      "global_load_lds_dwordx4 v10, s[88:89]\n"
      "s_add_u32 m0, m0, 1024\n"
      "v_xor_b32 v10, 3168, v11\n"
      "global_load_lds_dwordx4 v10, s[88:89]\n"
      "s_add_u32 m0, m0, 1024\n"
      "v_xor_b32 v10, 4224, v11\n"
      "global_load_lds_dwordx4 v10, s[88:89]\n"
      "s_add_u32 m0, m0, 1024\n"
      "v_xor_b32 v10, 5280, v11\n"
      "global_load_lds_dwordx4 v10, s[88:89]\n"
      "s_add_u32 m0, m0, 1024\n"
      "v_xor_b32 v10, 6336, v11\n"
      "global_load_lds_dwordx4 v10, s[88:89]\n"
      "s_add_u32 m0, m0, 1024\n"
      "v_xor_b32 v10, 7392, v11\n"
      "global_load_lds_dwordx4 v10, s[88:89]\n"
      "s_add_u32 s88, s88, 32768\n"
      "s_addc_u32 s89, s89, 0\n"
      "v_add_u32 v4, 0, v2\n"
      "v_xor_b32 v4, v4, v1\n"
      "v_lshlrev_b32 v4, 4, v4\n"
      "v_lshl_add_u32 v13, v1, 9, v4\n"
      "v_add_u32 v4, 4, v2\n"
      "v_xor_b32 v4, v4, v1\n"
      "v_lshlrev_b32 v4, 4, v4\n"
      "v_lshl_add_u32 v14, v1, 9, v4\n"
      "v_add_u32 v4, 8, v2\n"
      "v_xor_b32 v4, v4, v1\n"
      "v_lshlrev_b32 v4, 4, v4\n"
      "v_lshl_add_u32 v15, v1, 9, v4\n"
      "v_add_u32 v4, 12, v2\n"
      "v_xor_b32 v4, v4, v1\n"
      "v_lshlrev_b32 v4, 4, v4\n"
      "v_lshl_add_u32 v16, v1, 9, v4\n"
      "v_add_u32 v4, 16, v2\n"
      "v_xor_b32 v4, v4, v1\n"
      "v_lshlrev_b32 v4, 4, v4\n"
      "v_lshl_add_u32 v17, v1, 9, v4\n"
      "v_add_u32 v4, 20, v2\n"
      "v_xor_b32 v4, v4, v1\n"
      "v_lshlrev_b32 v4, 4, v4\n"
      "v_lshl_add_u32 v18, v1, 9, v4\n"
      "v_add_u32 v4, 24, v2\n"
      "v_xor_b32 v4, v4, v1\n"
      "v_lshlrev_b32 v4, 4, v4\n"
      "v_lshl_add_u32 v19, v1, 9, v4\n"
      "v_add_u32 v4, 28, v2\n"
      "v_xor_b32 v4, v4, v1\n"
      "v_lshlrev_b32 v4, 4, v4\n"
      "v_lshl_add_u32 v20, v1, 9, v4\n"
      "v_lshrrev_b32 v4, 2, v1\n"
      "v_and_b32 v5, 3, v1\n"
      "v_lshl_add_u32 v6, v2, 2, v4\n"
      "v_lshlrev_b32 v6, 9, v6\n"
      "v_lshl_add_u32 v6, v5, 3, v6\n"
      "v_add_u32 v6, 0x10000, v6\n"
      "v_and_b32 v5, 1, v2\n"
      "v_lshl_add_u32 v5, v5, 2, v4\n"
      "v_xor_b32 v4, 0, v5\n"
      "v_lshl_add_u32 v21, v4, 5, v6\n"
      "v_xor_b32 v4, 1, v5\n"
      "v_lshl_add_u32 v22, v4, 5, v6\n"
      "v_xor_b32 v4, 2, v5\n"
      "v_lshl_add_u32 v23, v4, 5, v6\n"
      "v_xor_b32 v4, 3, v5\n"
      "v_lshl_add_u32 v24, v4, 5, v6\n"
      "v_xor_b32 v4, 4, v5\n"
      "v_lshl_add_u32 v25, v4, 5, v6\n"
      "v_xor_b32 v4, 5, v5\n"
      "v_lshl_add_u32 v26, v4, 5, v6\n"
      "v_xor_b32 v4, 6, v5\n"
      "v_lshl_add_u32 v27, v4, 5, v6\n"
      "v_xor_b32 v4, 7, v5\n"
      "v_lshl_add_u32 v28, v4, 5, v6\n"
      "v_mov_b32 v34, 0x3f803f80\n"
      "v_mov_b32 v35, 0x3f803f80\n"
      "v_mov_b32 v36, 0x3f803f80\n"
      "v_mov_b32 v37, 0x3f803f80\n"
      "v_mov_b32 v38, 0\n"
      "v_mov_b32 v39, 0\n"
      "v_mov_b32 v40, 0\n"
      "v_mov_b32 v41, 0\n"
      "v_mov_b32 v42, 0\n"
      "v_mov_b32 v43, 0\n"
      "v_mov_b32 v44, 0\n"
      "v_mov_b32 v45, 0\n"
      "v_mov_b32 v46, 0\n"
      "v_mov_b32 v47, 0\n"
      "v_mov_b32 v48, 0\n"
      "v_mov_b32 v49, 0\n"
      "v_mov_b32 v32, 0xff800000\n"
      "v_accvgpr_write_b32 a0, 0\n"
      "v_accvgpr_write_b32 a1, 0\n"
      "v_accvgpr_write_b32 a2, 0\n"
      "v_accvgpr_write_b32 a3, 0\n"
      "v_accvgpr_write_b32 a4, 0\n"
      "v_accvgpr_write_b32 a5, 0\n"
      "v_accvgpr_write_b32 a6, 0\n"
      "v_accvgpr_write_b32 a7, 0\n"
      "v_accvgpr_write_b32 a8, 0\n"
      "v_accvgpr_write_b32 a9, 0\n"
      "v_accvgpr_write_b32 a10, 0\n"
      "v_accvgpr_write_b32 a11, 0\n"
      "v_accvgpr_write_b32 a12, 0\n"
      "v_accvgpr_write_b32 a13, 0\n"
      "v_accvgpr_write_b32 a14, 0\n"
      "v_accvgpr_write_b32 a15, 0\n"
      "v_accvgpr_write_b32 a16, 0\n"
      "v_accvgpr_write_b32 a17, 0\n"
      "v_accvgpr_write_b32 a18, 0\n"
      "v_accvgpr_write_b32 a19, 0\n"
      "v_accvgpr_write_b32 a20, 0\n"
      "v_accvgpr_write_b32 a21, 0\n"
      "v_accvgpr_write_b32 a22, 0\n"
      "v_accvgpr_write_b32 a23, 0\n"
      "v_accvgpr_write_b32 a24, 0\n"
      "v_accvgpr_write_b32 a25, 0\n"
      "v_accvgpr_write_b32 a26, 0\n"
      "v_accvgpr_write_b32 a27, 0\n"
      "v_accvgpr_write_b32 a28, 0\n"
      "v_accvgpr_write_b32 a29, 0\n"
      "v_accvgpr_write_b32 a30, 0\n"
      "v_accvgpr_write_b32 a31, 0\n"
      "v_accvgpr_write_b32 a32, 0\n"
      "v_accvgpr_write_b32 a33, 0\n"
      "v_accvgpr_write_b32 a34, 0\n"
      "v_accvgpr_write_b32 a35, 0\n"
      "v_accvgpr_write_b32 a36, 0\n"
      "v_accvgpr_write_b32 a37, 0\n"
      "v_accvgpr_write_b32 a38, 0\n"
      "v_accvgpr_write_b32 a39, 0\n"
      "v_accvgpr_write_b32 a40, 0\n"
      "v_accvgpr_write_b32 a41, 0\n"
      "v_accvgpr_write_b32 a42, 0\n"
      "v_accvgpr_write_b32 a43, 0\n"
      "v_accvgpr_write_b32 a44, 0\n"
      "v_accvgpr_write_b32 a45, 0\n"
      "v_accvgpr_write_b32 a46, 0\n"
      "v_accvgpr_write_b32 a47, 0\n"
      "v_accvgpr_write_b32 a48, 0\n"
      "v_accvgpr_write_b32 a49, 0\n"
      "v_accvgpr_write_b32 a50, 0\n"
      "v_accvgpr_write_b32 a51, 0\n"
      "v_accvgpr_write_b32 a52, 0\n"
      "v_accvgpr_write_b32 a53, 0\n"
      "v_accvgpr_write_b32 a54, 0\n"
      "v_accvgpr_write_b32 a55, 0\n"
      "v_accvgpr_write_b32 a56, 0\n"
      "v_accvgpr_write_b32 a57, 0\n"
      "v_accvgpr_write_b32 a58, 0\n"
      "v_accvgpr_write_b32 a59, 0\n"
      "v_accvgpr_write_b32 a60, 0\n"
      "v_accvgpr_write_b32 a61, 0\n"
      "v_accvgpr_write_b32 a62, 0\n"
      "v_accvgpr_write_b32 a63, 0\n"
      "v_accvgpr_write_b32 a64, 0\n"
      "v_accvgpr_write_b32 a65, 0\n"
      "v_accvgpr_write_b32 a66, 0\n"
      "v_accvgpr_write_b32 a67, 0\n"
      "v_accvgpr_write_b32 a68, 0\n"
      "v_accvgpr_write_b32 a69, 0\n"
      "v_accvgpr_write_b32 a70, 0\n"
      "v_accvgpr_write_b32 a71, 0\n"
      "v_accvgpr_write_b32 a72, 0\n"
      "v_accvgpr_write_b32 a73, 0\n"
      "v_accvgpr_write_b32 a74, 0\n"
      "v_accvgpr_write_b32 a75, 0\n"
      "v_accvgpr_write_b32 a76, 0\n"
      "v_accvgpr_write_b32 a77, 0\n"
      "v_accvgpr_write_b32 a78, 0\n"
      "v_accvgpr_write_b32 a79, 0\n"
      "v_accvgpr_write_b32 a80, 0\n"
      "v_accvgpr_write_b32 a81, 0\n"
      "v_accvgpr_write_b32 a82, 0\n"
      "v_accvgpr_write_b32 a83, 0\n"
      "v_accvgpr_write_b32 a84, 0\n"
      "v_accvgpr_write_b32 a85, 0\n"
      "v_accvgpr_write_b32 a86, 0\n"
      "v_accvgpr_write_b32 a87, 0\n"
      "v_accvgpr_write_b32 a88, 0\n"
      "v_accvgpr_write_b32 a89, 0\n"
      "v_accvgpr_write_b32 a90, 0\n"
      "v_accvgpr_write_b32 a91, 0\n"
      "v_accvgpr_write_b32 a92, 0\n"
      "v_accvgpr_write_b32 a93, 0\n"
      "v_accvgpr_write_b32 a94, 0\n"
      "v_accvgpr_write_b32 a95, 0\n"
      "v_accvgpr_write_b32 a96, 0\n"
      "v_accvgpr_write_b32 a97, 0\n"
      "v_accvgpr_write_b32 a98, 0\n"
      "v_accvgpr_write_b32 a99, 0\n"
      "v_accvgpr_write_b32 a100, 0\n"
      "v_accvgpr_write_b32 a101, 0\n"
      "v_accvgpr_write_b32 a102, 0\n"
      "v_accvgpr_write_b32 a103, 0\n"
      "v_accvgpr_write_b32 a104, 0\n"
      "v_accvgpr_write_b32 a105, 0\n"
      "v_accvgpr_write_b32 a106, 0\n"
      "v_accvgpr_write_b32 a107, 0\n"
      "v_accvgpr_write_b32 a108, 0\n"
      "v_accvgpr_write_b32 a109, 0\n"
      "v_accvgpr_write_b32 a110, 0\n"
      "v_accvgpr_write_b32 a111, 0\n"
      "v_accvgpr_write_b32 a112, 0\n"
      "v_accvgpr_write_b32 a113, 0\n"
      "v_accvgpr_write_b32 a114, 0\n"
      "v_accvgpr_write_b32 a115, 0\n"
      "v_accvgpr_write_b32 a116, 0\n"
      "v_accvgpr_write_b32 a117, 0\n"
      "v_accvgpr_write_b32 a118, 0\n"
      "v_accvgpr_write_b32 a119, 0\n"
      "v_accvgpr_write_b32 a120, 0\n"
      "v_accvgpr_write_b32 a121, 0\n"
      "v_accvgpr_write_b32 a122, 0\n"
      "v_accvgpr_write_b32 a123, 0\n"
      "v_accvgpr_write_b32 a124, 0\n"
      "v_accvgpr_write_b32 a125, 0\n"
      "v_accvgpr_write_b32 a126, 0\n"
      "v_accvgpr_write_b32 a127, 0\n"
      "v_accvgpr_write_b32 a128, 0\n"
      "v_accvgpr_write_b32 a129, 0\n"
      "v_accvgpr_write_b32 a130, 0\n"
      "v_accvgpr_write_b32 a131, 0\n"
      "v_accvgpr_write_b32 a132, 0\n"
      "v_accvgpr_write_b32 a133, 0\n"
      "v_accvgpr_write_b32 a134, 0\n"
      "v_accvgpr_write_b32 a135, 0\n"
      "v_accvgpr_write_b32 a136, 0\n"
      "v_accvgpr_write_b32 a137, 0\n"
      "v_accvgpr_write_b32 a138, 0\n"
      "v_accvgpr_write_b32 a139, 0\n"
      "v_accvgpr_write_b32 a140, 0\n"
      "v_accvgpr_write_b32 a141, 0\n"
      "v_accvgpr_write_b32 a142, 0\n"
      "v_accvgpr_write_b32 a143, 0\n"
      "v_accvgpr_write_b32 a144, 0\n"
      "v_accvgpr_write_b32 a145, 0\n"
      "v_accvgpr_write_b32 a146, 0\n"
      "v_accvgpr_write_b32 a147, 0\n"
      "v_accvgpr_write_b32 a148, 0\n"
      "v_accvgpr_write_b32 a149, 0\n"
      "v_accvgpr_write_b32 a150, 0\n"
      "v_accvgpr_write_b32 a151, 0\n"
      "v_accvgpr_write_b32 a152, 0\n"
      "v_accvgpr_write_b32 a153, 0\n"
      "v_accvgpr_write_b32 a154, 0\n"
      "v_accvgpr_write_b32 a155, 0\n"
      "v_accvgpr_write_b32 a156, 0\n"
      "v_accvgpr_write_b32 a157, 0\n"
      "v_accvgpr_write_b32 a158, 0\n"
      "v_accvgpr_write_b32 a159, 0\n"
      "v_accvgpr_write_b32 a160, 0\n"
      "v_accvgpr_write_b32 a161, 0\n"
      "v_accvgpr_write_b32 a162, 0\n"
      "v_accvgpr_write_b32 a163, 0\n"
      "v_accvgpr_write_b32 a164, 0\n"
      "v_accvgpr_write_b32 a165, 0\n"
      "v_accvgpr_write_b32 a166, 0\n"
      "v_accvgpr_write_b32 a167, 0\n"
      "v_accvgpr_write_b32 a168, 0\n"
      "v_accvgpr_write_b32 a169, 0\n"
      "v_accvgpr_write_b32 a170, 0\n"
      "v_accvgpr_write_b32 a171, 0\n"
      "v_accvgpr_write_b32 a172, 0\n"
      "v_accvgpr_write_b32 a173, 0\n"
      "v_accvgpr_write_b32 a174, 0\n"
      "v_accvgpr_write_b32 a175, 0\n"
      "v_accvgpr_write_b32 a176, 0\n"
      "v_accvgpr_write_b32 a177, 0\n"
      "v_accvgpr_write_b32 a178, 0\n"
      "v_accvgpr_write_b32 a179, 0\n"
      "v_accvgpr_write_b32 a180, 0\n"
      "v_accvgpr_write_b32 a181, 0\n"
      "v_accvgpr_write_b32 a182, 0\n"
      "v_accvgpr_write_b32 a183, 0\n"
      "v_accvgpr_write_b32 a184, 0\n"
      "v_accvgpr_write_b32 a185, 0\n"
      "v_accvgpr_write_b32 a186, 0\n"
      "v_accvgpr_write_b32 a187, 0\n"
      "v_accvgpr_write_b32 a188, 0\n"
      "v_accvgpr_write_b32 a189, 0\n"
      "v_accvgpr_write_b32 a190, 0\n"
      "v_accvgpr_write_b32 a191, 0\n"
      "v_accvgpr_write_b32 a192, 0\n"
      "v_accvgpr_write_b32 a193, 0\n"
      "v_accvgpr_write_b32 a194, 0\n"
      "v_accvgpr_write_b32 a195, 0\n"
      "v_accvgpr_write_b32 a196, 0\n"
      "v_accvgpr_write_b32 a197, 0\n"
      "v_accvgpr_write_b32 a198, 0\n"
      "v_accvgpr_write_b32 a199, 0\n"
      "v_accvgpr_write_b32 a200, 0\n"
      "v_accvgpr_write_b32 a201, 0\n"
      "v_accvgpr_write_b32 a202, 0\n"
      "v_accvgpr_write_b32 a203, 0\n"
      "s_waitcnt vmcnt(8)\n"
      "s_add_u32 m0, %4, 65536\n"
      "v_xor_b32 v10, 0, v12\n"
      "global_load_lds_dwordx4 v10, s[90:91]\n"
      "s_add_u32 m0, m0, 1024\n"
      "v_xor_b32 v10, 1088, v12\n"
      "global_load_lds_dwordx4 v10, s[90:91]\n"
      "s_add_u32 m0, m0, 1024\n"
      "v_xor_b32 v10, 2176, v12\n"
      "global_load_lds_dwordx4 v10, s[90:91]\n"
      "s_add_u32 m0, m0, 1024\n"
      "v_xor_b32 v10, 3264, v12\n"
      "global_load_lds_dwordx4 v10, s[90:91]\n"
      "s_add_u32 m0, m0, 1024\n"
      "v_xor_b32 v10, 4096, v12\n"
      "global_load_lds_dwordx4 v10, s[90:91]\n"
      "s_add_u32 m0, m0, 1024\n"
      "v_xor_b32 v10, 5184, v12\n"
      "global_load_lds_dwordx4 v10, s[90:91]\n"
      "s_add_u32 m0, m0, 1024\n"
      "v_xor_b32 v10, 6272, v12\n"
      "global_load_lds_dwordx4 v10, s[90:91]\n"
      "s_add_u32 m0, m0, 1024\n"
      "v_xor_b32 v10, 7360, v12\n"
      "global_load_lds_dwordx4 v10, s[90:91]\n"
      "s_add_u32 s90, s90, 32768\n"
      "s_addc_u32 s91, s91, 0\n"
      "s_add_u32 m0, %4, 32768\n"
      "v_xor_b32 v10, 0, v11\n"
      "global_load_lds_dwordx4 v10, s[88:89]\n"
      "s_add_u32 m0, m0, 1024\n"
      "v_xor_b32 v10, 1056, v11\n"
      "global_load_lds_dwordx4 v10, s[88:89]\n"
      "s_add_u32 m0, m0, 1024\n"
      "v_xor_b32 v10, 2112, v11\n"
      "global_load_lds_dwordx4 v10, s[88:89]\n"
      "s_add_u32 m0, m0, 1024\n"
      "v_xor_b32 v10, 3168, v11\n"
      "global_load_lds_dwordx4 v10, s[88:89]\n"
      "s_add_u32 m0, m0, 1024\n"
      "v_xor_b32 v10, 4224, v11\n"
      "global_load_lds_dwordx4 v10, s[88:89]\n"
      "s_add_u32 m0, m0, 1024\n"
      "v_xor_b32 v10, 5280, v11\n"
      "global_load_lds_dwordx4 v10, s[88:89]\n"
      "s_add_u32 m0, m0, 1024\n"
      "v_xor_b32 v10, 6336, v11\n"
      "global_load_lds_dwordx4 v10, s[88:89]\n"
      "s_add_u32 m0, m0, 1024\n"
      "v_xor_b32 v10, 7392, v11\n"
      "global_load_lds_dwordx4 v10, s[88:89]\n"
      "s_add_u32 s88, s88, 32768\n"
      "s_addc_u32 s89, s89, 0\n"
      "v_lshlrev_b32 v3, 16, v106\n"
      "v_and_b32 v4, 0xffff0000, v106\n"
      "v_mul_f32 v3, 0x3db8aa3b, v3\n"
      "v_mul_f32 v4, 0x3db8aa3b, v4\n"
      "v_cvt_pk_bf16_f32 v202, v3, v4\n"
      "v_lshlrev_b32 v3, 16, v107\n"
      "v_and_b32 v4, 0xffff0000, v107\n"
      "v_mul_f32 v3, 0x3db8aa3b, v3\n"
      "v_mul_f32 v4, 0x3db8aa3b, v4\n"
      "v_cvt_pk_bf16_f32 v203, v3, v4\n"
      "v_lshlrev_b32 v3, 16, v108\n"
      "v_and_b32 v4, 0xffff0000, v108\n"
      "v_mul_f32 v3, 0x3db8aa3b, v3\n"
      "v_mul_f32 v4, 0x3db8aa3b, v4\n"
      "v_cvt_pk_bf16_f32 v204, v3, v4\n"
      "v_lshlrev_b32 v3, 16, v109\n"
      "v_and_b32 v4, 0xffff0000, v109\n"
      "v_mul_f32 v3, 0x3db8aa3b, v3\n"
      "v_mul_f32 v4, 0x3db8aa3b, v4\n"
      "v_cvt_pk_bf16_f32 v205, v3, v4\n"
      "v_lshlrev_b32 v3, 16, v110\n"
      "v_and_b32 v4, 0xffff0000, v110\n"
      "v_mul_f32 v3, 0x3db8aa3b, v3\n"
      "v_mul_f32 v4, 0x3db8aa3b, v4\n"
      "v_cvt_pk_bf16_f32 v206, v3, v4\n"
      "v_lshlrev_b32 v3, 16, v111\n"
      "v_and_b32 v4, 0xffff0000, v111\n"
      "v_mul_f32 v3, 0x3db8aa3b, v3\n"
      "v_mul_f32 v4, 0x3db8aa3b, v4\n"
      "v_cvt_pk_bf16_f32 v207, v3, v4\n"
      "v_lshlrev_b32 v3, 16, v112\n"
      "v_and_b32 v4, 0xffff0000, v112\n"
      "v_mul_f32 v3, 0x3db8aa3b, v3\n"
      "v_mul_f32 v4, 0x3db8aa3b, v4\n"
      "v_cvt_pk_bf16_f32 v208, v3, v4\n"
      "v_lshlrev_b32 v3, 16, v113\n"
      "v_and_b32 v4, 0xffff0000, v113\n"
      "v_mul_f32 v3, 0x3db8aa3b, v3\n"
      "v_mul_f32 v4, 0x3db8aa3b, v4\n"
      "v_cvt_pk_bf16_f32 v209, v3, v4\n"
      "v_lshlrev_b32 v3, 16, v114\n"
      "v_and_b32 v4, 0xffff0000, v114\n"
      "v_mul_f32 v3, 0x3db8aa3b, v3\n"
      "v_mul_f32 v4, 0x3db8aa3b, v4\n"
      "v_cvt_pk_bf16_f32 v210, v3, v4\n"
      "v_lshlrev_b32 v3, 16, v115\n"
      "v_and_b32 v4, 0xffff0000, v115\n"
      "v_mul_f32 v3, 0x3db8aa3b, v3\n"
      "v_mul_f32 v4, 0x3db8aa3b, v4\n"
      "v_cvt_pk_bf16_f32 v211, v3, v4\n"
      "v_lshlrev_b32 v3, 16, v116\n"
      "v_and_b32 v4, 0xffff0000, v116\n"
      "v_mul_f32 v3, 0x3db8aa3b, v3\n"
      "v_mul_f32 v4, 0x3db8aa3b, v4\n"
      "v_cvt_pk_bf16_f32 v212, v3, v4\n"
      "v_lshlrev_b32 v3, 16, v117\n"
      "v_and_b32 v4, 0xffff0000, v117\n"
      "v_mul_f32 v3, 0x3db8aa3b, v3\n"
      "v_mul_f32 v4, 0x3db8aa3b, v4\n"
      "v_cvt_pk_bf16_f32 v213, v3, v4\n"
      "v_lshlrev_b32 v3, 16, v118\n"
      "v_and_b32 v4, 0xffff0000, v118\n"
      "v_mul_f32 v3, 0x3db8aa3b, v3\n"
      "v_mul_f32 v4, 0x3db8aa3b, v4\n"
      "v_cvt_pk_bf16_f32 v214, v3, v4\n"
      "v_lshlrev_b32 v3, 16, v119\n"
      "v_and_b32 v4, 0xffff0000, v119\n"
      "v_mul_f32 v3, 0x3db8aa3b, v3\n"
      "v_mul_f32 v4, 0x3db8aa3b, v4\n"
      "v_cvt_pk_bf16_f32 v215, v3, v4\n"
      "v_lshlrev_b32 v3, 16, v120\n"
      "v_and_b32 v4, 0xffff0000, v120\n"
      "v_mul_f32 v3, 0x3db8aa3b, v3\n"
      "v_mul_f32 v4, 0x3db8aa3b, v4\n"
      "v_cvt_pk_bf16_f32 v216, v3, v4\n"
      "v_lshlrev_b32 v3, 16, v121\n"
      "v_and_b32 v4, 0xffff0000, v121\n"
      "v_mul_f32 v3, 0x3db8aa3b, v3\n"
      "v_mul_f32 v4, 0x3db8aa3b, v4\n"
      "v_cvt_pk_bf16_f32 v217, v3, v4\n"
      "v_lshlrev_b32 v3, 16, v122\n"
      "v_and_b32 v4, 0xffff0000, v122\n"
      "v_mul_f32 v3, 0x3db8aa3b, v3\n"
      "v_mul_f32 v4, 0x3db8aa3b, v4\n"
      "v_cvt_pk_bf16_f32 v218, v3, v4\n"
      "v_lshlrev_b32 v3, 16, v123\n"
      "v_and_b32 v4, 0xffff0000, v123\n"
      "v_mul_f32 v3, 0x3db8aa3b, v3\n"
      "v_mul_f32 v4, 0x3db8aa3b, v4\n"
      "v_cvt_pk_bf16_f32 v219, v3, v4\n"
      "v_lshlrev_b32 v3, 16, v124\n"
      "v_and_b32 v4, 0xffff0000, v124\n"
      "v_mul_f32 v3, 0x3db8aa3b, v3\n"
      "v_mul_f32 v4, 0x3db8aa3b, v4\n"
      "v_cvt_pk_bf16_f32 v220, v3, v4\n"
      "v_lshlrev_b32 v3, 16, v125\n"
      "v_and_b32 v4, 0xffff0000, v125\n"
      "v_mul_f32 v3, 0x3db8aa3b, v3\n"
      "v_mul_f32 v4, 0x3db8aa3b, v4\n"
      "v_cvt_pk_bf16_f32 v221, v3, v4\n"
      "v_lshlrev_b32 v3, 16, v126\n"
      "v_and_b32 v4, 0xffff0000, v126\n"
      "v_mul_f32 v3, 0x3db8aa3b, v3\n"
      "v_mul_f32 v4, 0x3db8aa3b, v4\n"
      "v_cvt_pk_bf16_f32 v222, v3, v4\n"
      "v_lshlrev_b32 v3, 16, v127\n"
      "v_and_b32 v4, 0xffff0000, v127\n"
      "v_mul_f32 v3, 0x3db8aa3b, v3\n"
      "v_mul_f32 v4, 0x3db8aa3b, v4\n"
      "v_cvt_pk_bf16_f32 v223, v3, v4\n"
      "v_lshlrev_b32 v3, 16, v128\n"
      "v_and_b32 v4, 0xffff0000, v128\n"
      "v_mul_f32 v3, 0x3db8aa3b, v3\n"
      "v_mul_f32 v4, 0x3db8aa3b, v4\n"
      "v_cvt_pk_bf16_f32 v224, v3, v4\n"
      "v_lshlrev_b32 v3, 16, v129\n"
      "v_and_b32 v4, 0xffff0000, v129\n"
      "v_mul_f32 v3, 0x3db8aa3b, v3\n"
      "v_mul_f32 v4, 0x3db8aa3b, v4\n"
      "v_cvt_pk_bf16_f32 v225, v3, v4\n"
      "v_lshlrev_b32 v3, 16, v130\n"
      "v_and_b32 v4, 0xffff0000, v130\n"
      "v_mul_f32 v3, 0x3db8aa3b, v3\n"
      "v_mul_f32 v4, 0x3db8aa3b, v4\n"
      "v_cvt_pk_bf16_f32 v226, v3, v4\n"
      "v_lshlrev_b32 v3, 16, v131\n"
      "v_and_b32 v4, 0xffff0000, v131\n"
      "v_mul_f32 v3, 0x3db8aa3b, v3\n"
      "v_mul_f32 v4, 0x3db8aa3b, v4\n"
      "v_cvt_pk_bf16_f32 v227, v3, v4\n"
      "v_lshlrev_b32 v3, 16, v132\n"
      "v_and_b32 v4, 0xffff0000, v132\n"
      "v_mul_f32 v3, 0x3db8aa3b, v3\n"
      "v_mul_f32 v4, 0x3db8aa3b, v4\n"
      "v_cvt_pk_bf16_f32 v228, v3, v4\n"
      "v_lshlrev_b32 v3, 16, v133\n"
      "v_and_b32 v4, 0xffff0000, v133\n"
      "v_mul_f32 v3, 0x3db8aa3b, v3\n"
      "v_mul_f32 v4, 0x3db8aa3b, v4\n"
      "v_cvt_pk_bf16_f32 v229, v3, v4\n"
      "v_lshlrev_b32 v3, 16, v134\n"
      "v_and_b32 v4, 0xffff0000, v134\n"
      "v_mul_f32 v3, 0x3db8aa3b, v3\n"
      "v_mul_f32 v4, 0x3db8aa3b, v4\n"
      "v_cvt_pk_bf16_f32 v230, v3, v4\n"
      "v_lshlrev_b32 v3, 16, v135\n"
      "v_and_b32 v4, 0xffff0000, v135\n"
      "v_mul_f32 v3, 0x3db8aa3b, v3\n"
      "v_mul_f32 v4, 0x3db8aa3b, v4\n"
      "v_cvt_pk_bf16_f32 v231, v3, v4\n"
      "v_lshlrev_b32 v3, 16, v136\n"
      "v_and_b32 v4, 0xffff0000, v136\n"
      "v_mul_f32 v3, 0x3db8aa3b, v3\n"
      "v_mul_f32 v4, 0x3db8aa3b, v4\n"
      "v_cvt_pk_bf16_f32 v232, v3, v4\n"
      "v_lshlrev_b32 v3, 16, v137\n"
      "v_and_b32 v4, 0xffff0000, v137\n"
      "v_mul_f32 v3, 0x3db8aa3b, v3\n"
      "v_mul_f32 v4, 0x3db8aa3b, v4\n"
      "v_cvt_pk_bf16_f32 v233, v3, v4\n"
      "v_lshlrev_b32 v3, 16, v138\n"
      "v_and_b32 v4, 0xffff0000, v138\n"
      "v_mul_f32 v3, 0x3db8aa3b, v3\n"
      "v_mul_f32 v4, 0x3db8aa3b, v4\n"
      "v_cvt_pk_bf16_f32 v234, v3, v4\n"
      "v_lshlrev_b32 v3, 16, v139\n"
      "v_and_b32 v4, 0xffff0000, v139\n"
      "v_mul_f32 v3, 0x3db8aa3b, v3\n"
      "v_mul_f32 v4, 0x3db8aa3b, v4\n"
      "v_cvt_pk_bf16_f32 v235, v3, v4\n"
      "v_lshlrev_b32 v3, 16, v140\n"
      "v_and_b32 v4, 0xffff0000, v140\n"
      "v_mul_f32 v3, 0x3db8aa3b, v3\n"
      "v_mul_f32 v4, 0x3db8aa3b, v4\n"
      "v_cvt_pk_bf16_f32 v236, v3, v4\n"
      "v_lshlrev_b32 v3, 16, v141\n"
      "v_and_b32 v4, 0xffff0000, v141\n"
      "v_mul_f32 v3, 0x3db8aa3b, v3\n"
      "v_mul_f32 v4, 0x3db8aa3b, v4\n"
      "v_cvt_pk_bf16_f32 v237, v3, v4\n"
      "v_lshlrev_b32 v3, 16, v142\n"
      "v_and_b32 v4, 0xffff0000, v142\n"
      "v_mul_f32 v3, 0x3db8aa3b, v3\n"
      "v_mul_f32 v4, 0x3db8aa3b, v4\n"
      "v_cvt_pk_bf16_f32 v238, v3, v4\n"
      "v_lshlrev_b32 v3, 16, v143\n"
      "v_and_b32 v4, 0xffff0000, v143\n"
      "v_mul_f32 v3, 0x3db8aa3b, v3\n"
      "v_mul_f32 v4, 0x3db8aa3b, v4\n"
      "v_cvt_pk_bf16_f32 v239, v3, v4\n"
      "v_lshlrev_b32 v3, 16, v144\n"
      "v_and_b32 v4, 0xffff0000, v144\n"
      "v_mul_f32 v3, 0x3db8aa3b, v3\n"
      "v_mul_f32 v4, 0x3db8aa3b, v4\n"
      "v_cvt_pk_bf16_f32 v240, v3, v4\n"
      "v_lshlrev_b32 v3, 16, v145\n"
      "v_and_b32 v4, 0xffff0000, v145\n"
      "v_mul_f32 v3, 0x3db8aa3b, v3\n"
      "v_mul_f32 v4, 0x3db8aa3b, v4\n"
      "v_cvt_pk_bf16_f32 v241, v3, v4\n"
      "v_lshlrev_b32 v3, 16, v146\n"
      "v_and_b32 v4, 0xffff0000, v146\n"
      "v_mul_f32 v3, 0x3db8aa3b, v3\n"
      "v_mul_f32 v4, 0x3db8aa3b, v4\n"
      "v_cvt_pk_bf16_f32 v242, v3, v4\n"
      "v_lshlrev_b32 v3, 16, v147\n"
      "v_and_b32 v4, 0xffff0000, v147\n"
      "v_mul_f32 v3, 0x3db8aa3b, v3\n"
      "v_mul_f32 v4, 0x3db8aa3b, v4\n"
      "v_cvt_pk_bf16_f32 v243, v3, v4\n"
      "v_lshlrev_b32 v3, 16, v148\n"
      "v_and_b32 v4, 0xffff0000, v148\n"
      "v_mul_f32 v3, 0x3db8aa3b, v3\n"
      "v_mul_f32 v4, 0x3db8aa3b, v4\n"
      "v_cvt_pk_bf16_f32 v244, v3, v4\n"
      "v_lshlrev_b32 v3, 16, v149\n"
      "v_and_b32 v4, 0xffff0000, v149\n"
      "v_mul_f32 v3, 0x3db8aa3b, v3\n"
      "v_mul_f32 v4, 0x3db8aa3b, v4\n"
      "v_cvt_pk_bf16_f32 v245, v3, v4\n"
      "v_lshlrev_b32 v3, 16, v150\n"
      "v_and_b32 v4, 0xffff0000, v150\n"
      "v_mul_f32 v3, 0x3db8aa3b, v3\n"
      "v_mul_f32 v4, 0x3db8aa3b, v4\n"
      "v_cvt_pk_bf16_f32 v3, v3, v4\n"
      "v_accvgpr_write_b32 a204, v3\n"
      "v_lshlrev_b32 v3, 16, v151\n"
      "v_and_b32 v4, 0xffff0000, v151\n"
      "v_mul_f32 v3, 0x3db8aa3b, v3\n"
      "v_mul_f32 v4, 0x3db8aa3b, v4\n"
      "v_cvt_pk_bf16_f32 v3, v3, v4\n"
      "v_accvgpr_write_b32 a205, v3\n"
      "v_lshlrev_b32 v3, 16, v152\n"
      "v_and_b32 v4, 0xffff0000, v152\n"
      "v_mul_f32 v3, 0x3db8aa3b, v3\n"
      "v_mul_f32 v4, 0x3db8aa3b, v4\n"
      "v_cvt_pk_bf16_f32 v3, v3, v4\n"
      "v_accvgpr_write_b32 a206, v3\n"
      "v_lshlrev_b32 v3, 16, v153\n"
      "v_and_b32 v4, 0xffff0000, v153\n"
      "v_mul_f32 v3, 0x3db8aa3b, v3\n"
      "v_mul_f32 v4, 0x3db8aa3b, v4\n"
      "v_cvt_pk_bf16_f32 v3, v3, v4\n"
      "v_accvgpr_write_b32 a207, v3\n"
      "v_lshlrev_b32 v3, 16, v154\n"
      "v_and_b32 v4, 0xffff0000, v154\n"
      "v_mul_f32 v3, 0x3db8aa3b, v3\n"
      "v_mul_f32 v4, 0x3db8aa3b, v4\n"
      "v_cvt_pk_bf16_f32 v3, v3, v4\n"
      "v_accvgpr_write_b32 a208, v3\n"
      "v_lshlrev_b32 v3, 16, v155\n"
      "v_and_b32 v4, 0xffff0000, v155\n"
      "v_mul_f32 v3, 0x3db8aa3b, v3\n"
      "v_mul_f32 v4, 0x3db8aa3b, v4\n"
      "v_cvt_pk_bf16_f32 v3, v3, v4\n"
      "v_accvgpr_write_b32 a209, v3\n"
      "v_lshlrev_b32 v3, 16, v156\n"
      "v_and_b32 v4, 0xffff0000, v156\n"
      "v_mul_f32 v3, 0x3db8aa3b, v3\n"
      "v_mul_f32 v4, 0x3db8aa3b, v4\n"
      "v_cvt_pk_bf16_f32 v3, v3, v4\n"
      "v_accvgpr_write_b32 a210, v3\n"
      "v_lshlrev_b32 v3, 16, v157\n"
      "v_and_b32 v4, 0xffff0000, v157\n"
      "v_mul_f32 v3, 0x3db8aa3b, v3\n"
      "v_mul_f32 v4, 0x3db8aa3b, v4\n"
      "v_cvt_pk_bf16_f32 v3, v3, v4\n"
      "v_accvgpr_write_b32 a211, v3\n"
      "v_lshlrev_b32 v3, 16, v158\n"
      "v_and_b32 v4, 0xffff0000, v158\n"
      "v_mul_f32 v3, 0x3db8aa3b, v3\n"
      "v_mul_f32 v4, 0x3db8aa3b, v4\n"
      "v_cvt_pk_bf16_f32 v3, v3, v4\n"
      "v_accvgpr_write_b32 a212, v3\n"
      "v_lshlrev_b32 v3, 16, v159\n"
      "v_and_b32 v4, 0xffff0000, v159\n"
      "v_mul_f32 v3, 0x3db8aa3b, v3\n"
      "v_mul_f32 v4, 0x3db8aa3b, v4\n"
      "v_cvt_pk_bf16_f32 v3, v3, v4\n"
      "v_accvgpr_write_b32 a213, v3\n"
      "v_lshlrev_b32 v3, 16, v160\n"
      "v_and_b32 v4, 0xffff0000, v160\n"
      "v_mul_f32 v3, 0x3db8aa3b, v3\n"
      "v_mul_f32 v4, 0x3db8aa3b, v4\n"
      "v_cvt_pk_bf16_f32 v3, v3, v4\n"
      "v_accvgpr_write_b32 a214, v3\n"
      "v_lshlrev_b32 v3, 16, v161\n"
      "v_and_b32 v4, 0xffff0000, v161\n"
      "v_mul_f32 v3, 0x3db8aa3b, v3\n"
      "v_mul_f32 v4, 0x3db8aa3b, v4\n"
      "v_cvt_pk_bf16_f32 v3, v3, v4\n"
      "v_accvgpr_write_b32 a215, v3\n"
      "v_lshlrev_b32 v3, 16, v162\n"
      "v_and_b32 v4, 0xffff0000, v162\n"
      "v_mul_f32 v3, 0x3db8aa3b, v3\n"
      "v_mul_f32 v4, 0x3db8aa3b, v4\n"
      "v_cvt_pk_bf16_f32 v3, v3, v4\n"
      "v_accvgpr_write_b32 a216, v3\n"
      "v_lshlrev_b32 v3, 16, v163\n"
      "v_and_b32 v4, 0xffff0000, v163\n"
      "v_mul_f32 v3, 0x3db8aa3b, v3\n"
      "v_mul_f32 v4, 0x3db8aa3b, v4\n"
      "v_cvt_pk_bf16_f32 v3, v3, v4\n"
      "v_accvgpr_write_b32 a217, v3\n"
      "v_lshlrev_b32 v3, 16, v164\n"
      "v_and_b32 v4, 0xffff0000, v164\n"
      "v_mul_f32 v3, 0x3db8aa3b, v3\n"
      "v_mul_f32 v4, 0x3db8aa3b, v4\n"
      "v_cvt_pk_bf16_f32 v3, v3, v4\n"
      "v_accvgpr_write_b32 a218, v3\n"
      "v_lshlrev_b32 v3, 16, v165\n"
      "v_and_b32 v4, 0xffff0000, v165\n"
      "v_mul_f32 v3, 0x3db8aa3b, v3\n"
      "v_mul_f32 v4, 0x3db8aa3b, v4\n"
      "v_cvt_pk_bf16_f32 v3, v3, v4\n"
      "v_accvgpr_write_b32 a219, v3\n"
      "v_lshlrev_b32 v3, 16, v166\n"
      "v_and_b32 v4, 0xffff0000, v166\n"
      "v_mul_f32 v3, 0x3db8aa3b, v3\n"
      "v_mul_f32 v4, 0x3db8aa3b, v4\n"
      "v_cvt_pk_bf16_f32 v3, v3, v4\n"
      "v_accvgpr_write_b32 a220, v3\n"
      "v_lshlrev_b32 v3, 16, v167\n"
      "v_and_b32 v4, 0xffff0000, v167\n"
      "v_mul_f32 v3, 0x3db8aa3b, v3\n"
      "v_mul_f32 v4, 0x3db8aa3b, v4\n"
      "v_cvt_pk_bf16_f32 v3, v3, v4\n"
      "v_accvgpr_write_b32 a221, v3\n"
      "v_lshlrev_b32 v3, 16, v168\n"
      "v_and_b32 v4, 0xffff0000, v168\n"
      "v_mul_f32 v3, 0x3db8aa3b, v3\n"
      "v_mul_f32 v4, 0x3db8aa3b, v4\n"
      "v_cvt_pk_bf16_f32 v3, v3, v4\n"
      "v_accvgpr_write_b32 a222, v3\n"
      "v_lshlrev_b32 v3, 16, v169\n"
      "v_and_b32 v4, 0xffff0000, v169\n"
      "v_mul_f32 v3, 0x3db8aa3b, v3\n"
      "v_mul_f32 v4, 0x3db8aa3b, v4\n"
      "v_cvt_pk_bf16_f32 v3, v3, v4\n"
      "v_accvgpr_write_b32 a223, v3\n"
      "v_lshlrev_b32 v3, 16, v170\n"
      "v_and_b32 v4, 0xffff0000, v170\n"
      "v_mul_f32 v3, 0x3db8aa3b, v3\n"
      "v_mul_f32 v4, 0x3db8aa3b, v4\n"
      "v_cvt_pk_bf16_f32 v3, v3, v4\n"
      "v_accvgpr_write_b32 a224, v3\n"
      "v_lshlrev_b32 v3, 16, v171\n"
      "v_and_b32 v4, 0xffff0000, v171\n"
      "v_mul_f32 v3, 0x3db8aa3b, v3\n"
      "v_mul_f32 v4, 0x3db8aa3b, v4\n"
      "v_cvt_pk_bf16_f32 v3, v3, v4\n"
      "v_accvgpr_write_b32 a225, v3\n"
      "v_lshlrev_b32 v3, 16, v172\n"
      "v_and_b32 v4, 0xffff0000, v172\n"
      "v_mul_f32 v3, 0x3db8aa3b, v3\n"
      "v_mul_f32 v4, 0x3db8aa3b, v4\n"
      "v_cvt_pk_bf16_f32 v3, v3, v4\n"
      "v_accvgpr_write_b32 a226, v3\n"
      "v_lshlrev_b32 v3, 16, v173\n"
      "v_and_b32 v4, 0xffff0000, v173\n"
      "v_mul_f32 v3, 0x3db8aa3b, v3\n"
      "v_mul_f32 v4, 0x3db8aa3b, v4\n"
      "v_cvt_pk_bf16_f32 v3, v3, v4\n"
      "v_accvgpr_write_b32 a227, v3\n"
      "v_lshlrev_b32 v3, 16, v174\n"
      "v_and_b32 v4, 0xffff0000, v174\n"
      "v_mul_f32 v3, 0x3db8aa3b, v3\n"
      "v_mul_f32 v4, 0x3db8aa3b, v4\n"
      "v_cvt_pk_bf16_f32 v3, v3, v4\n"
      "v_accvgpr_write_b32 a228, v3\n"
      "v_lshlrev_b32 v3, 16, v175\n"
      "v_and_b32 v4, 0xffff0000, v175\n"
      "v_mul_f32 v3, 0x3db8aa3b, v3\n"
      "v_mul_f32 v4, 0x3db8aa3b, v4\n"
      "v_cvt_pk_bf16_f32 v3, v3, v4\n"
      "v_accvgpr_write_b32 a229, v3\n"
      "v_lshlrev_b32 v3, 16, v176\n"
      "v_and_b32 v4, 0xffff0000, v176\n"
      "v_mul_f32 v3, 0x3db8aa3b, v3\n"
      "v_mul_f32 v4, 0x3db8aa3b, v4\n"
      "v_cvt_pk_bf16_f32 v3, v3, v4\n"
      "v_accvgpr_write_b32 a230, v3\n"
      "v_lshlrev_b32 v3, 16, v177\n"
      "v_and_b32 v4, 0xffff0000, v177\n"
      "v_mul_f32 v3, 0x3db8aa3b, v3\n"
      "v_mul_f32 v4, 0x3db8aa3b, v4\n"
      "v_cvt_pk_bf16_f32 v3, v3, v4\n"
      "v_accvgpr_write_b32 a231, v3\n"
      "v_lshlrev_b32 v3, 16, v178\n"
      "v_and_b32 v4, 0xffff0000, v178\n"
      "v_mul_f32 v3, 0x3db8aa3b, v3\n"
      "v_mul_f32 v4, 0x3db8aa3b, v4\n"
      "v_cvt_pk_bf16_f32 v3, v3, v4\n"
      "v_accvgpr_write_b32 a232, v3\n"
      "v_lshlrev_b32 v3, 16, v179\n"
      "v_and_b32 v4, 0xffff0000, v179\n"
      "v_mul_f32 v3, 0x3db8aa3b, v3\n"
      "v_mul_f32 v4, 0x3db8aa3b, v4\n"
      "v_cvt_pk_bf16_f32 v3, v3, v4\n"
      "v_accvgpr_write_b32 a233, v3\n"
      "v_lshlrev_b32 v3, 16, v180\n"
      "v_and_b32 v4, 0xffff0000, v180\n"
      "v_mul_f32 v3, 0x3db8aa3b, v3\n"
      "v_mul_f32 v4, 0x3db8aa3b, v4\n"
      "v_cvt_pk_bf16_f32 v3, v3, v4\n"
      "v_accvgpr_write_b32 a234, v3\n"
      "v_lshlrev_b32 v3, 16, v181\n"
      "v_and_b32 v4, 0xffff0000, v181\n"
      "v_mul_f32 v3, 0x3db8aa3b, v3\n"
      "v_mul_f32 v4, 0x3db8aa3b, v4\n"
      "v_cvt_pk_bf16_f32 v3, v3, v4\n"
      "v_accvgpr_write_b32 a235, v3\n"
      "v_lshlrev_b32 v3, 16, v182\n"
      "v_and_b32 v4, 0xffff0000, v182\n"
      "v_mul_f32 v3, 0x3db8aa3b, v3\n"
      "v_mul_f32 v4, 0x3db8aa3b, v4\n"
      "v_cvt_pk_bf16_f32 v3, v3, v4\n"
      "v_accvgpr_write_b32 a236, v3\n"
      "v_lshlrev_b32 v3, 16, v183\n"
      "v_and_b32 v4, 0xffff0000, v183\n"
      "v_mul_f32 v3, 0x3db8aa3b, v3\n"
      "v_mul_f32 v4, 0x3db8aa3b, v4\n"
      "v_cvt_pk_bf16_f32 v3, v3, v4\n"
      "v_accvgpr_write_b32 a237, v3\n"
      "v_lshlrev_b32 v3, 16, v184\n"
      "v_and_b32 v4, 0xffff0000, v184\n"
      "v_mul_f32 v3, 0x3db8aa3b, v3\n"
      "v_mul_f32 v4, 0x3db8aa3b, v4\n"
      "v_cvt_pk_bf16_f32 v3, v3, v4\n"
      "v_accvgpr_write_b32 a238, v3\n"
      "v_lshlrev_b32 v3, 16, v185\n"
      "v_and_b32 v4, 0xffff0000, v185\n"
      "v_mul_f32 v3, 0x3db8aa3b, v3\n"
      "v_mul_f32 v4, 0x3db8aa3b, v4\n"
      "v_cvt_pk_bf16_f32 v3, v3, v4\n"
      "v_accvgpr_write_b32 a239, v3\n"
      "v_lshlrev_b32 v3, 16, v186\n"
      "v_and_b32 v4, 0xffff0000, v186\n"
      "v_mul_f32 v3, 0x3db8aa3b, v3\n"
      "v_mul_f32 v4, 0x3db8aa3b, v4\n"
      "v_cvt_pk_bf16_f32 v3, v3, v4\n"
      "v_accvgpr_write_b32 a240, v3\n"
      "v_lshlrev_b32 v3, 16, v187\n"
      "v_and_b32 v4, 0xffff0000, v187\n"
      "v_mul_f32 v3, 0x3db8aa3b, v3\n"
      "v_mul_f32 v4, 0x3db8aa3b, v4\n"
      "v_cvt_pk_bf16_f32 v3, v3, v4\n"
      "v_accvgpr_write_b32 a241, v3\n"
      "v_lshlrev_b32 v3, 16, v188\n"
      "v_and_b32 v4, 0xffff0000, v188\n"
      "v_mul_f32 v3, 0x3db8aa3b, v3\n"
      "v_mul_f32 v4, 0x3db8aa3b, v4\n"
      "v_cvt_pk_bf16_f32 v3, v3, v4\n"
      "v_accvgpr_write_b32 a242, v3\n"
      "v_lshlrev_b32 v3, 16, v189\n"
      "v_and_b32 v4, 0xffff0000, v189\n"
      "v_mul_f32 v3, 0x3db8aa3b, v3\n"
      "v_mul_f32 v4, 0x3db8aa3b, v4\n"
      "v_cvt_pk_bf16_f32 v3, v3, v4\n"
      "v_accvgpr_write_b32 a243, v3\n"
      "v_lshlrev_b32 v3, 16, v190\n"
      "v_and_b32 v4, 0xffff0000, v190\n"
      "v_mul_f32 v3, 0x3db8aa3b, v3\n"
      "v_mul_f32 v4, 0x3db8aa3b, v4\n"
      "v_cvt_pk_bf16_f32 v3, v3, v4\n"
      "v_accvgpr_write_b32 a244, v3\n"
      "v_lshlrev_b32 v3, 16, v191\n"
      "v_and_b32 v4, 0xffff0000, v191\n"
      "v_mul_f32 v3, 0x3db8aa3b, v3\n"
      "v_mul_f32 v4, 0x3db8aa3b, v4\n"
      "v_cvt_pk_bf16_f32 v3, v3, v4\n"
      "v_accvgpr_write_b32 a245, v3\n"
      "v_lshlrev_b32 v3, 16, v192\n"
      "v_and_b32 v4, 0xffff0000, v192\n"
      "v_mul_f32 v3, 0x3db8aa3b, v3\n"
      "v_mul_f32 v4, 0x3db8aa3b, v4\n"
      "v_cvt_pk_bf16_f32 v3, v3, v4\n"
      "v_accvgpr_write_b32 a246, v3\n"
      "v_lshlrev_b32 v3, 16, v193\n"
      "v_and_b32 v4, 0xffff0000, v193\n"
      "v_mul_f32 v3, 0x3db8aa3b, v3\n"
      "v_mul_f32 v4, 0x3db8aa3b, v4\n"
      "v_cvt_pk_bf16_f32 v3, v3, v4\n"
      "v_accvgpr_write_b32 a247, v3\n"
      "v_lshlrev_b32 v3, 16, v194\n"
      "v_and_b32 v4, 0xffff0000, v194\n"
      "v_mul_f32 v3, 0x3db8aa3b, v3\n"
      "v_mul_f32 v4, 0x3db8aa3b, v4\n"
      "v_cvt_pk_bf16_f32 v3, v3, v4\n"
      "v_accvgpr_write_b32 a248, v3\n"
      "v_lshlrev_b32 v3, 16, v195\n"
      "v_and_b32 v4, 0xffff0000, v195\n"
      "v_mul_f32 v3, 0x3db8aa3b, v3\n"
      "v_mul_f32 v4, 0x3db8aa3b, v4\n"
      "v_cvt_pk_bf16_f32 v3, v3, v4\n"
      "v_accvgpr_write_b32 a249, v3\n"
      "v_lshlrev_b32 v3, 16, v196\n"
      "v_and_b32 v4, 0xffff0000, v196\n"
      "v_mul_f32 v3, 0x3db8aa3b, v3\n"
      "v_mul_f32 v4, 0x3db8aa3b, v4\n"
      "v_cvt_pk_bf16_f32 v3, v3, v4\n"
      "v_accvgpr_write_b32 a250, v3\n"
      "v_lshlrev_b32 v3, 16, v197\n"
      "v_and_b32 v4, 0xffff0000, v197\n"
      "v_mul_f32 v3, 0x3db8aa3b, v3\n"
      "v_mul_f32 v4, 0x3db8aa3b, v4\n"
      "v_cvt_pk_bf16_f32 v3, v3, v4\n"
      "v_accvgpr_write_b32 a251, v3\n"
      "v_lshlrev_b32 v3, 16, v198\n"
      "v_and_b32 v4, 0xffff0000, v198\n"
      "v_mul_f32 v3, 0x3db8aa3b, v3\n"
      "v_mul_f32 v4, 0x3db8aa3b, v4\n"
      "v_cvt_pk_bf16_f32 v3, v3, v4\n"
      "v_accvgpr_write_b32 a252, v3\n"
      "v_lshlrev_b32 v3, 16, v199\n"
      "v_and_b32 v4, 0xffff0000, v199\n"
      "v_mul_f32 v3, 0x3db8aa3b, v3\n"
      "v_mul_f32 v4, 0x3db8aa3b, v4\n"
      "v_cvt_pk_bf16_f32 v3, v3, v4\n"
      "v_accvgpr_write_b32 a253, v3\n"
      "v_lshlrev_b32 v3, 16, v200\n"
      "v_and_b32 v4, 0xffff0000, v200\n"
      "v_mul_f32 v3, 0x3db8aa3b, v3\n"
      "v_mul_f32 v4, 0x3db8aa3b, v4\n"
      "v_cvt_pk_bf16_f32 v3, v3, v4\n"
      "v_accvgpr_write_b32 a254, v3\n"
      "v_lshlrev_b32 v3, 16, v201\n"
      "v_and_b32 v4, 0xffff0000, v201\n"
      "v_mul_f32 v3, 0x3db8aa3b, v3\n"
      "v_mul_f32 v4, 0x3db8aa3b, v4\n"
      "v_cvt_pk_bf16_f32 v3, v3, v4\n"
      "v_accvgpr_write_b32 a255, v3\n"
      "s_waitcnt vmcnt(16)\n"
      "s_nop 4\n"
      "s_barrier\n"
      "ds_read_b128 v[50:53], v13 offset:0\n"
      "ds_read_b128 v[54:57], v13 offset:8192\n"
      "ds_read_b128 v[58:61], v13 offset:16384\n"
      "ds_read_b128 v[62:65], v13 offset:24576\n"
      "ds_read_b128 v[66:69], v14 offset:0\n"
      "ds_read_b128 v[70:73], v14 offset:8192\n"
      "s_waitcnt lgkmcnt(4)\n"
      "v_mfma_f32_16x16x32_bf16 v[106:109], v[50:53], v[202:205], v[38:41]\n"
      "ds_read_b128 v[74:77], v14 offset:16384\n"
      "v_mfma_f32_16x16x32_bf16 v[122:125], v[50:53], v[234:237], v[42:45]\n"
      "v_mfma_f32_16x16x32_bf16 v[138:141], v[50:53], a[224:227], v[46:49]\n"
      "v_mfma_f32_16x16x32_bf16 v[110:113], v[54:57], v[202:205], v[38:41]\n"
      "ds_read_b128 v[78:81], v14 offset:24576\n"
      "v_mfma_f32_16x16x32_bf16 v[126:129], v[54:57], v[234:237], v[42:45]\n"
      "v_mfma_f32_16x16x32_bf16 v[142:145], v[54:57], a[224:227], v[46:49]\n"
      "s_waitcnt lgkmcnt(4)\n"
      "v_mfma_f32_16x16x32_bf16 v[114:117], v[58:61], v[202:205], v[38:41]\n"
      "ds_read_b128 v[50:53], v15 offset:0\n"
      "v_mfma_f32_16x16x32_bf16 v[130:133], v[58:61], v[234:237], v[42:45]\n"
      "v_mfma_f32_16x16x32_bf16 v[146:149], v[58:61], a[224:227], v[46:49]\n"
      "v_mfma_f32_16x16x32_bf16 v[118:121], v[62:65], v[202:205], v[38:41]\n"
      "ds_read_b128 v[54:57], v15 offset:8192\n"
      "v_mfma_f32_16x16x32_bf16 v[134:137], v[62:65], v[234:237], v[42:45]\n"
      "v_mfma_f32_16x16x32_bf16 v[150:153], v[62:65], a[224:227], v[46:49]\n"
      "s_waitcnt lgkmcnt(4)\n"
      "v_mfma_f32_16x16x32_bf16 v[106:109], v[66:69], v[206:209], v[106:109]\n"
      "ds_read_b128 v[58:61], v15 offset:16384\n"
      "v_mfma_f32_16x16x32_bf16 v[122:125], v[66:69], v[238:241], v[122:125]\n"
      "v_mfma_f32_16x16x32_bf16 v[138:141], v[66:69], a[228:231], v[138:141]\n"
      "v_mfma_f32_16x16x32_bf16 v[110:113], v[70:73], v[206:209], v[110:113]\n"
      "ds_read_b128 v[62:65], v15 offset:24576\n"
      "v_mfma_f32_16x16x32_bf16 v[126:129], v[70:73], v[238:241], v[126:129]\n"
      "v_mfma_f32_16x16x32_bf16 v[142:145], v[70:73], a[228:231], v[142:145]\n"
      "s_waitcnt lgkmcnt(4)\n"
      "v_mfma_f32_16x16x32_bf16 v[114:117], v[74:77], v[206:209], v[114:117]\n"
      "ds_read_b128 v[66:69], v16 offset:0\n"
      "v_mfma_f32_16x16x32_bf16 v[130:133], v[74:77], v[238:241], v[130:133]\n"
      "v_mfma_f32_16x16x32_bf16 v[146:149], v[74:77], a[228:231], v[146:149]\n"
      "v_mfma_f32_16x16x32_bf16 v[118:121], v[78:81], v[206:209], v[118:121]\n"
      "ds_read_b128 v[70:73], v16 offset:8192\n"
      "v_mfma_f32_16x16x32_bf16 v[134:137], v[78:81], v[238:241], v[134:137]\n"
      "v_mfma_f32_16x16x32_bf16 v[150:153], v[78:81], a[228:231], v[150:153]\n"
      "s_waitcnt lgkmcnt(4)\n"
      "v_mfma_f32_16x16x32_bf16 v[106:109], v[50:53], v[210:213], v[106:109]\n"
      "ds_read_b128 v[74:77], v16 offset:16384\n"
      "v_mfma_f32_16x16x32_bf16 v[122:125], v[50:53], v[242:245], v[122:125]\n"
      "v_mfma_f32_16x16x32_bf16 v[138:141], v[50:53], a[232:235], v[138:141]\n"
      "v_mfma_f32_16x16x32_bf16 v[110:113], v[54:57], v[210:213], v[110:113]\n"
      "ds_read_b128 v[78:81], v16 offset:24576\n"
      "v_mfma_f32_16x16x32_bf16 v[126:129], v[54:57], v[242:245], v[126:129]\n"
      "v_mfma_f32_16x16x32_bf16 v[142:145], v[54:57], a[232:235], v[142:145]\n"
      "s_waitcnt lgkmcnt(4)\n"
      "v_mfma_f32_16x16x32_bf16 v[114:117], v[58:61], v[210:213], v[114:117]\n"
      "ds_read_b128 v[50:53], v17 offset:0\n"
      "v_mfma_f32_16x16x32_bf16 v[130:133], v[58:61], v[242:245], v[130:133]\n"
      "v_mfma_f32_16x16x32_bf16 v[146:149], v[58:61], a[232:235], v[146:149]\n"
      "v_mfma_f32_16x16x32_bf16 v[118:121], v[62:65], v[210:213], v[118:121]\n"
      "ds_read_b128 v[54:57], v17 offset:8192\n"
      "v_mfma_f32_16x16x32_bf16 v[134:137], v[62:65], v[242:245], v[134:137]\n"
      "v_mfma_f32_16x16x32_bf16 v[150:153], v[62:65], a[232:235], v[150:153]\n"
      "s_waitcnt lgkmcnt(4)\n"
      "v_mfma_f32_16x16x32_bf16 v[106:109], v[66:69], v[214:217], v[106:109]\n"
      "ds_read_b128 v[58:61], v17 offset:16384\n"
      "v_mfma_f32_16x16x32_bf16 v[122:125], v[66:69], a[204:207], v[122:125]\n"
      "v_mfma_f32_16x16x32_bf16 v[138:141], v[66:69], a[236:239], v[138:141]\n"
      "v_mfma_f32_16x16x32_bf16 v[110:113], v[70:73], v[214:217], v[110:113]\n"
      "ds_read_b128 v[62:65], v17 offset:24576\n"
      "v_mfma_f32_16x16x32_bf16 v[126:129], v[70:73], a[204:207], v[126:129]\n"
      "v_mfma_f32_16x16x32_bf16 v[142:145], v[70:73], a[236:239], v[142:145]\n"
      "s_waitcnt lgkmcnt(4)\n"
      "v_mfma_f32_16x16x32_bf16 v[114:117], v[74:77], v[214:217], v[114:117]\n"
      "ds_read_b128 v[66:69], v18 offset:0\n"
      "v_mfma_f32_16x16x32_bf16 v[130:133], v[74:77], a[204:207], v[130:133]\n"
      "v_mfma_f32_16x16x32_bf16 v[146:149], v[74:77], a[236:239], v[146:149]\n"
      "v_mfma_f32_16x16x32_bf16 v[118:121], v[78:81], v[214:217], v[118:121]\n"
      "ds_read_b128 v[70:73], v18 offset:8192\n"
      "v_mfma_f32_16x16x32_bf16 v[134:137], v[78:81], a[204:207], v[134:137]\n"
      "v_mfma_f32_16x16x32_bf16 v[150:153], v[78:81], a[236:239], v[150:153]\n"
      "s_waitcnt lgkmcnt(4)\n"
      "v_mfma_f32_16x16x32_bf16 v[106:109], v[50:53], v[218:221], v[106:109]\n"
      "ds_read_b128 v[74:77], v18 offset:16384\n"
      "v_mfma_f32_16x16x32_bf16 v[122:125], v[50:53], a[208:211], v[122:125]\n"
      "v_mfma_f32_16x16x32_bf16 v[138:141], v[50:53], a[240:243], v[138:141]\n"
      "v_mfma_f32_16x16x32_bf16 v[110:113], v[54:57], v[218:221], v[110:113]\n"
      "ds_read_b128 v[78:81], v18 offset:24576\n"
      "v_mfma_f32_16x16x32_bf16 v[126:129], v[54:57], a[208:211], v[126:129]\n"
      "v_mfma_f32_16x16x32_bf16 v[142:145], v[54:57], a[240:243], v[142:145]\n"
      "s_waitcnt lgkmcnt(4)\n"
      "v_mfma_f32_16x16x32_bf16 v[114:117], v[58:61], v[218:221], v[114:117]\n"
      "ds_read_b128 v[50:53], v19 offset:0\n"
      "v_mfma_f32_16x16x32_bf16 v[130:133], v[58:61], a[208:211], v[130:133]\n"
      "v_mfma_f32_16x16x32_bf16 v[146:149], v[58:61], a[240:243], v[146:149]\n"
      "v_mfma_f32_16x16x32_bf16 v[118:121], v[62:65], v[218:221], v[118:121]\n"
      "ds_read_b128 v[54:57], v19 offset:8192\n"
      "v_mfma_f32_16x16x32_bf16 v[134:137], v[62:65], a[208:211], v[134:137]\n"
      "v_mfma_f32_16x16x32_bf16 v[150:153], v[62:65], a[240:243], v[150:153]\n"
      "s_waitcnt lgkmcnt(4)\n"
      "v_mfma_f32_16x16x32_bf16 v[106:109], v[66:69], v[222:225], v[106:109]\n"
      "ds_read_b128 v[58:61], v19 offset:16384\n"
      "v_mfma_f32_16x16x32_bf16 v[122:125], v[66:69], a[212:215], v[122:125]\n"
      "v_mfma_f32_16x16x32_bf16 v[138:141], v[66:69], a[244:247], v[138:141]\n"
      "v_mfma_f32_16x16x32_bf16 v[110:113], v[70:73], v[222:225], v[110:113]\n"
      "ds_read_b128 v[62:65], v19 offset:24576\n"
      "v_mfma_f32_16x16x32_bf16 v[126:129], v[70:73], a[212:215], v[126:129]\n"
      "v_mfma_f32_16x16x32_bf16 v[142:145], v[70:73], a[244:247], v[142:145]\n"
      "s_waitcnt lgkmcnt(4)\n"
      "v_mfma_f32_16x16x32_bf16 v[114:117], v[74:77], v[222:225], v[114:117]\n"
      "ds_read_b128 v[66:69], v20 offset:0\n"
      "v_mfma_f32_16x16x32_bf16 v[130:133], v[74:77], a[212:215], v[130:133]\n"
      "v_mfma_f32_16x16x32_bf16 v[146:149], v[74:77], a[244:247], v[146:149]\n"
      "v_mfma_f32_16x16x32_bf16 v[118:121], v[78:81], v[222:225], v[118:121]\n"
      "ds_read_b128 v[70:73], v20 offset:8192\n"
      "v_mfma_f32_16x16x32_bf16 v[134:137], v[78:81], a[212:215], v[134:137]\n"
      "v_mfma_f32_16x16x32_bf16 v[150:153], v[78:81], a[244:247], v[150:153]\n"
      "s_waitcnt lgkmcnt(4)\n"
      "v_mfma_f32_16x16x32_bf16 v[106:109], v[50:53], v[226:229], v[106:109]\n"
      "ds_read_b128 v[74:77], v20 offset:16384\n"
      "v_mfma_f32_16x16x32_bf16 v[122:125], v[50:53], a[216:219], v[122:125]\n"
      "v_mfma_f32_16x16x32_bf16 v[138:141], v[50:53], a[248:251], v[138:141]\n"
      "v_mfma_f32_16x16x32_bf16 v[110:113], v[54:57], v[226:229], v[110:113]\n"
      "ds_read_b128 v[78:81], v20 offset:24576\n"
      "v_mfma_f32_16x16x32_bf16 v[126:129], v[54:57], a[216:219], v[126:129]\n"
      "v_mfma_f32_16x16x32_bf16 v[142:145], v[54:57], a[248:251], v[142:145]\n"
      "s_waitcnt lgkmcnt(4)\n"
      "v_mfma_f32_16x16x32_bf16 v[114:117], v[58:61], v[226:229], v[114:117]\n"
      "v_mfma_f32_16x16x32_bf16 v[130:133], v[58:61], a[216:219], v[130:133]\n"
      "v_mfma_f32_16x16x32_bf16 v[146:149], v[58:61], a[248:251], v[146:149]\n"
      "v_mfma_f32_16x16x32_bf16 v[118:121], v[62:65], v[226:229], v[118:121]\n"
      "v_mfma_f32_16x16x32_bf16 v[134:137], v[62:65], a[216:219], v[134:137]\n"
      "v_mfma_f32_16x16x32_bf16 v[150:153], v[62:65], a[248:251], v[150:153]\n"
      "s_waitcnt lgkmcnt(2)\n"
      "v_mfma_f32_16x16x32_bf16 v[106:109], v[66:69], v[230:233], v[106:109]\n"
      "v_mfma_f32_16x16x32_bf16 v[122:125], v[66:69], a[220:223], v[122:125]\n"
      "v_mfma_f32_16x16x32_bf16 v[138:141], v[66:69], a[252:255], v[138:141]\n"
      "v_mfma_f32_16x16x32_bf16 v[110:113], v[70:73], v[230:233], v[110:113]\n"
      "v_mfma_f32_16x16x32_bf16 v[126:129], v[70:73], a[220:223], v[126:129]\n"
      "v_mfma_f32_16x16x32_bf16 v[142:145], v[70:73], a[252:255], v[142:145]\n"
      "s_waitcnt lgkmcnt(1)\n"
      "v_mfma_f32_16x16x32_bf16 v[114:117], v[74:77], v[230:233], v[114:117]\n"
      "v_mfma_f32_16x16x32_bf16 v[130:133], v[74:77], a[220:223], v[130:133]\n"
      "v_mfma_f32_16x16x32_bf16 v[146:149], v[74:77], a[252:255], v[146:149]\n"
      "s_waitcnt lgkmcnt(0)\n"
      "v_mfma_f32_16x16x32_bf16 v[118:121], v[78:81], v[230:233], v[118:121]\n"
      "v_mfma_f32_16x16x32_bf16 v[134:137], v[78:81], a[220:223], v[134:137]\n"
      "v_mfma_f32_16x16x32_bf16 v[150:153], v[78:81], a[252:255], v[150:153]\n"
      "v_max3_f32 v29, v106, v107, v108\n"
      "v_max3_f32 v29, v29, v109, v110\n"
      "v_max3_f32 v29, v29, v111, v112\n"
      "v_max3_f32 v29, v29, v113, v114\n"
      "v_max3_f32 v29, v29, v115, v116\n"
      "s_nop 0\n"
      "v_max3_f32 v29, v29, v117, v118\n"
      "v_max3_f32 v29, v29, v119, v120\n"
      "v_max_f32 v29, v29, v121\n"
      "v_mov_b32 v3, v29\n"
      "s_nop 1\n"
      "v_permlane32_swap_b32 v3, v29\n"
      "v_max_f32 v29, v29, v3\n"
      "v_mov_b32 v3, v29\n"
      "s_nop 1\n"
      "v_permlane16_swap_b32 v3, v29\n"
      "v_max_f32 v29, v29, v3\n"
      "v_max3_f32 v30, v122, v123, v124\n"
      "v_max3_f32 v30, v30, v125, v126\n"
      "v_max3_f32 v30, v30, v127, v128\n"
      "v_max3_f32 v30, v30, v129, v130\n"
      "v_max3_f32 v30, v30, v131, v132\n"
      "v_max3_f32 v30, v30, v133, v134\n"
      "v_max3_f32 v30, v30, v135, v136\n"
      "v_max_f32 v30, v30, v137\n"
      "v_mov_b32 v4, v30\n"
      "s_nop 1\n"
      "v_permlane32_swap_b32 v4, v30\n"
      "v_max_f32 v30, v30, v4\n"
      "v_mov_b32 v4, v30\n"
      "s_nop 1\n"
      "v_permlane16_swap_b32 v4, v30\n"
      "v_max_f32 v30, v30, v4\n"
      "v_max3_f32 v31, v138, v139, v140\n"
      "v_max3_f32 v31, v31, v141, v142\n"
      "v_max3_f32 v31, v31, v143, v144\n"
      "v_max3_f32 v31, v31, v145, v146\n"
      "v_max3_f32 v31, v31, v147, v148\n"
      "v_max3_f32 v31, v31, v149, v150\n"
      "v_max3_f32 v31, v31, v151, v152\n"
      "v_max_f32 v31, v31, v153\n"
      "v_mov_b32 v5, v31\n"
      "s_nop 1\n"
      "v_permlane32_swap_b32 v5, v31\n"
      "v_max_f32 v31, v31, v5\n"
      "v_mov_b32 v5, v31\n"
      "s_nop 1\n"
      "v_permlane16_swap_b32 v5, v31\n"
      "v_max_f32 v31, v31, v5\n"
      "s_branch SLOW0_%=\n"
      "BACK0_%=:\n"
      "s_waitcnt vmcnt(0)\n"
      "s_barrier\n"
      "s_mov_b32 s86, 7\n"
      "LOOP_%=:\n"
      "ds_read_b128 v[50:53], v13 offset:32768\n"
      "ds_read_b128 v[54:57], v13 offset:40960\n"
      "ds_read_b128 v[58:61], v13 offset:49152\n"
      "ds_read_b128 v[62:65], v13 offset:57344\n"
      "ds_read_b128 v[66:69], v14 offset:32768\n"
      "ds_read_b128 v[70:73], v14 offset:40960\n"
      "s_waitcnt lgkmcnt(4)\n"
      "v_mfma_f32_16x16x32_bf16 v[154:157], v[50:53], v[202:205], v[38:41]\n"
      "ds_read_b128 v[74:77], v14 offset:49152\n"
      "v_mfma_f32_16x16x32_bf16 v[170:173], v[50:53], v[234:237], v[42:45]\n"
      "s_add_u32 m0, %4, 0\n"
      "v_xor_b32 v10, 0, v11\n"
      "global_load_lds_dwordx4 v10, s[88:89]\n"
      "s_add_u32 m0, m0, 1024\n"
      "v_xor_b32 v10, 1056, v11\n"
      "global_load_lds_dwordx4 v10, s[88:89]\n"
      "v_mfma_f32_16x16x32_bf16 v[186:189], v[50:53], a[224:227], v[46:49]\n"
      "s_add_u32 m0, m0, 1024\n"
      "v_xor_b32 v10, 2112, v11\n"
      "global_load_lds_dwordx4 v10, s[88:89]\n"
      "s_add_u32 m0, m0, 1024\n"
      "v_xor_b32 v10, 3168, v11\n"
      "global_load_lds_dwordx4 v10, s[88:89]\n"
      "v_mfma_f32_16x16x32_bf16 v[158:161], v[54:57], v[202:205], v[38:41]\n"
      "ds_read_b128 v[78:81], v14 offset:57344\n"
      "v_mfma_f32_16x16x32_bf16 v[174:177], v[54:57], v[234:237], v[42:45]\n"
      "s_add_u32 m0, m0, 1024\n"
      "v_xor_b32 v10, 4224, v11\n"
      "global_load_lds_dwordx4 v10, s[88:89]\n"
      "s_add_u32 m0, m0, 1024\n"
      "v_xor_b32 v10, 5280, v11\n"
      "global_load_lds_dwordx4 v10, s[88:89]\n"
      "v_mfma_f32_16x16x32_bf16 v[190:193], v[54:57], a[224:227], v[46:49]\n"
      "s_add_u32 m0, m0, 1024\n"
      "v_xor_b32 v10, 6336, v11\n"
      "global_load_lds_dwordx4 v10, s[88:89]\n"
      "s_add_u32 m0, m0, 1024\n"
      "v_xor_b32 v10, 7392, v11\n"
      "global_load_lds_dwordx4 v10, s[88:89]\n"
      "s_waitcnt lgkmcnt(4)\n"
      "v_mfma_f32_16x16x32_bf16 v[162:165], v[58:61], v[202:205], v[38:41]\n"
      "ds_read_b128 v[50:53], v15 offset:32768\n"
      "v_mfma_f32_16x16x32_bf16 v[178:181], v[58:61], v[234:237], v[42:45]\n"
      "v_exp_f32 v106, v106\n"
      "v_mfma_f32_16x16x32_bf16 v[194:197], v[58:61], a[224:227], v[46:49]\n"
      "v_exp_f32 v107, v107\n"
      "v_mfma_f32_16x16x32_bf16 v[166:169], v[62:65], v[202:205], v[38:41]\n"
      "ds_read_b128 v[54:57], v15 offset:40960\n"
      "v_mfma_f32_16x16x32_bf16 v[182:185], v[62:65], v[234:237], v[42:45]\n"
      "v_exp_f32 v108, v108\n"
      "v_mfma_f32_16x16x32_bf16 v[198:201], v[62:65], a[224:227], v[46:49]\n"
      "v_exp_f32 v109, v109\n"
      "s_waitcnt lgkmcnt(4)\n"
      "v_mfma_f32_16x16x32_bf16 v[154:157], v[66:69], v[206:209], v[154:157]\n"
      "ds_read_b128 v[58:61], v15 offset:49152\n"
      "v_mfma_f32_16x16x32_bf16 v[170:173], v[66:69], v[238:241], v[170:173]\n"
      "v_exp_f32 v110, v110\n"
      "v_mfma_f32_16x16x32_bf16 v[186:189], v[66:69], a[228:231], v[186:189]\n"
      "v_exp_f32 v111, v111\n"
      "v_mfma_f32_16x16x32_bf16 v[158:161], v[70:73], v[206:209], v[158:161]\n"
      "ds_read_b128 v[62:65], v15 offset:57344\n"
      "v_mfma_f32_16x16x32_bf16 v[174:177], v[70:73], v[238:241], v[174:177]\n"
      "v_exp_f32 v112, v112\n"
      "v_mfma_f32_16x16x32_bf16 v[190:193], v[70:73], a[228:231], v[190:193]\n"
      "v_exp_f32 v113, v113\n"
      "s_waitcnt lgkmcnt(4)\n"
      "v_mfma_f32_16x16x32_bf16 v[162:165], v[74:77], v[206:209], v[162:165]\n"
      "ds_read_b128 v[66:69], v16 offset:32768\n"
      "v_mfma_f32_16x16x32_bf16 v[178:181], v[74:77], v[238:241], v[178:181]\n"
      "v_cvt_pk_bf16_f32 v82, v106, v107\n"
      "v_cvt_pk_bf16_f32 v83, v108, v109\n"
      "v_mfma_f32_16x16x32_bf16 v[194:197], v[74:77], a[228:231], v[194:197]\n"
      "v_cvt_pk_bf16_f32 v84, v110, v111\n"
      "v_cvt_pk_bf16_f32 v85, v112, v113\n"
      "v_mfma_f32_16x16x32_bf16 v[166:169], v[78:81], v[206:209], v[166:169]\n"
      "ds_read_b128 v[70:73], v16 offset:40960\n"
      "v_mfma_f32_16x16x32_bf16 v[182:185], v[78:81], v[238:241], v[182:185]\n"
      "v_exp_f32 v114, v114\n"
      "v_mfma_f32_16x16x32_bf16 v[198:201], v[78:81], a[228:231], v[198:201]\n"
      "v_exp_f32 v115, v115\n"
      "s_waitcnt lgkmcnt(4)\n"
      "v_mfma_f32_16x16x32_bf16 v[154:157], v[50:53], v[210:213], v[154:157]\n"
      "ds_read_b128 v[74:77], v16 offset:49152\n"
      "v_mfma_f32_16x16x32_bf16 v[170:173], v[50:53], v[242:245], v[170:173]\n"
      "v_exp_f32 v116, v116\n"
      "v_mfma_f32_16x16x32_bf16 v[186:189], v[50:53], a[232:235], v[186:189]\n"
      "v_exp_f32 v117, v117\n"
      "v_mfma_f32_16x16x32_bf16 v[158:161], v[54:57], v[210:213], v[158:161]\n"
      "ds_read_b128 v[78:81], v16 offset:57344\n"
      "v_mfma_f32_16x16x32_bf16 v[174:177], v[54:57], v[242:245], v[174:177]\n"
      "v_exp_f32 v118, v118\n"
      "v_mfma_f32_16x16x32_bf16 v[190:193], v[54:57], a[232:235], v[190:193]\n"
      "v_exp_f32 v119, v119\n"
      "s_waitcnt lgkmcnt(4)\n"
      "v_mfma_f32_16x16x32_bf16 v[162:165], v[58:61], v[210:213], v[162:165]\n"
      "ds_read_b128 v[50:53], v17 offset:32768\n"
      "v_mfma_f32_16x16x32_bf16 v[178:181], v[58:61], v[242:245], v[178:181]\n"
      "v_exp_f32 v120, v120\n"
      "v_mfma_f32_16x16x32_bf16 v[194:197], v[58:61], a[232:235], v[194:197]\n"
      "v_exp_f32 v121, v121\n"
      "v_mfma_f32_16x16x32_bf16 v[166:169], v[62:65], v[210:213], v[166:169]\n"
      "ds_read_b128 v[54:57], v17 offset:40960\n"
      "v_mfma_f32_16x16x32_bf16 v[182:185], v[62:65], v[242:245], v[182:185]\n"
      "v_cvt_pk_bf16_f32 v86, v114, v115\n"
      "v_cvt_pk_bf16_f32 v87, v116, v117\n"
      "v_mfma_f32_16x16x32_bf16 v[198:201], v[62:65], a[232:235], v[198:201]\n"
      "v_cvt_pk_bf16_f32 v88, v118, v119\n"
      "v_cvt_pk_bf16_f32 v89, v120, v121\n"
      "s_waitcnt lgkmcnt(4)\n"
      "v_mfma_f32_16x16x32_bf16 v[154:157], v[66:69], v[214:217], v[154:157]\n"
      "ds_read_b128 v[58:61], v17 offset:49152\n"
      "v_mfma_f32_16x16x32_bf16 v[170:173], v[66:69], a[204:207], v[170:173]\n"
      "v_exp_f32 v122, v122\n"
      "v_mfma_f32_16x16x32_bf16 v[186:189], v[66:69], a[236:239], v[186:189]\n"
      "v_exp_f32 v123, v123\n"
      "v_mfma_f32_16x16x32_bf16 v[158:161], v[70:73], v[214:217], v[158:161]\n"
      "ds_read_b128 v[62:65], v17 offset:57344\n"
      "v_mfma_f32_16x16x32_bf16 v[174:177], v[70:73], a[204:207], v[174:177]\n"
      "v_exp_f32 v124, v124\n"
      "v_mfma_f32_16x16x32_bf16 v[190:193], v[70:73], a[236:239], v[190:193]\n"
      "v_exp_f32 v125, v125\n"
      "s_waitcnt lgkmcnt(4)\n"
      "v_mfma_f32_16x16x32_bf16 v[162:165], v[74:77], v[214:217], v[162:165]\n"
      "ds_read_b128 v[66:69], v18 offset:32768\n"
      "v_mfma_f32_16x16x32_bf16 v[178:181], v[74:77], a[204:207], v[178:181]\n"
      "v_exp_f32 v126, v126\n"
      "v_mfma_f32_16x16x32_bf16 v[194:197], v[74:77], a[236:239], v[194:197]\n"
      "v_exp_f32 v127, v127\n"
      "v_mfma_f32_16x16x32_bf16 v[166:169], v[78:81], v[214:217], v[166:169]\n"
      "ds_read_b128 v[70:73], v18 offset:40960\n"
      "v_mfma_f32_16x16x32_bf16 v[182:185], v[78:81], a[204:207], v[182:185]\n"
      "v_exp_f32 v128, v128\n"
      "v_mfma_f32_16x16x32_bf16 v[198:201], v[78:81], a[236:239], v[198:201]\n"
      "v_exp_f32 v129, v129\n"
      "s_waitcnt lgkmcnt(4)\n"
      "v_mfma_f32_16x16x32_bf16 v[154:157], v[50:53], v[218:221], v[154:157]\n"
      "ds_read_b128 v[74:77], v18 offset:49152\n"
      "v_mfma_f32_16x16x32_bf16 v[170:173], v[50:53], a[208:211], v[170:173]\n"
      "v_cvt_pk_bf16_f32 v90, v122, v123\n"
      "v_cvt_pk_bf16_f32 v91, v124, v125\n"
      "v_mfma_f32_16x16x32_bf16 v[186:189], v[50:53], a[240:243], v[186:189]\n"
      "v_cvt_pk_bf16_f32 v92, v126, v127\n"
      "v_cvt_pk_bf16_f32 v93, v128, v129\n"
      "v_mfma_f32_16x16x32_bf16 v[158:161], v[54:57], v[218:221], v[158:161]\n"
      "ds_read_b128 v[78:81], v18 offset:57344\n"
      "v_mfma_f32_16x16x32_bf16 v[174:177], v[54:57], a[208:211], v[174:177]\n"
      "v_exp_f32 v130, v130\n"
      "v_mfma_f32_16x16x32_bf16 v[190:193], v[54:57], a[240:243], v[190:193]\n"
      "v_exp_f32 v131, v131\n"
      "s_waitcnt lgkmcnt(4)\n"
      "v_mfma_f32_16x16x32_bf16 v[162:165], v[58:61], v[218:221], v[162:165]\n"
      "ds_read_b128 v[50:53], v19 offset:32768\n"
      "v_mfma_f32_16x16x32_bf16 v[178:181], v[58:61], a[208:211], v[178:181]\n"
      "v_exp_f32 v132, v132\n"
      "v_mfma_f32_16x16x32_bf16 v[194:197], v[58:61], a[240:243], v[194:197]\n"
      "v_exp_f32 v133, v133\n"
      "v_mfma_f32_16x16x32_bf16 v[166:169], v[62:65], v[218:221], v[166:169]\n"
      "ds_read_b128 v[54:57], v19 offset:40960\n"
      "v_mfma_f32_16x16x32_bf16 v[182:185], v[62:65], a[208:211], v[182:185]\n"
      "v_exp_f32 v134, v134\n"
      "v_mfma_f32_16x16x32_bf16 v[198:201], v[62:65], a[240:243], v[198:201]\n"
      "v_exp_f32 v135, v135\n"
      "s_waitcnt lgkmcnt(4)\n"
      "v_mfma_f32_16x16x32_bf16 v[154:157], v[66:69], v[222:225], v[154:157]\n"
      "ds_read_b128 v[58:61], v19 offset:49152\n"
      "v_mfma_f32_16x16x32_bf16 v[170:173], v[66:69], a[212:215], v[170:173]\n"
      "v_exp_f32 v136, v136\n"
      "v_mfma_f32_16x16x32_bf16 v[186:189], v[66:69], a[244:247], v[186:189]\n"
      "v_exp_f32 v137, v137\n"
      "v_mfma_f32_16x16x32_bf16 v[158:161], v[70:73], v[222:225], v[158:161]\n"
      "ds_read_b128 v[62:65], v19 offset:57344\n"
      "v_mfma_f32_16x16x32_bf16 v[174:177], v[70:73], a[212:215], v[174:177]\n"
      "v_cvt_pk_bf16_f32 v94, v130, v131\n"
      "v_cvt_pk_bf16_f32 v95, v132, v133\n"
      "v_mfma_f32_16x16x32_bf16 v[190:193], v[70:73], a[244:247], v[190:193]\n"
      "v_cvt_pk_bf16_f32 v96, v134, v135\n"
      "v_cvt_pk_bf16_f32 v97, v136, v137\n"
      "s_waitcnt lgkmcnt(4)\n"
      "v_mfma_f32_16x16x32_bf16 v[162:165], v[74:77], v[222:225], v[162:165]\n"
      "ds_read_b128 v[66:69], v20 offset:32768\n"
      "v_mfma_f32_16x16x32_bf16 v[178:181], v[74:77], a[212:215], v[178:181]\n"
      "v_exp_f32 v138, v138\n"
      "v_mfma_f32_16x16x32_bf16 v[194:197], v[74:77], a[244:247], v[194:197]\n"
      "v_exp_f32 v139, v139\n"
      "v_mfma_f32_16x16x32_bf16 v[166:169], v[78:81], v[222:225], v[166:169]\n"
      "ds_read_b128 v[70:73], v20 offset:40960\n"
      "v_mfma_f32_16x16x32_bf16 v[182:185], v[78:81], a[212:215], v[182:185]\n"
      "v_exp_f32 v140, v140\n"
      "v_mfma_f32_16x16x32_bf16 v[198:201], v[78:81], a[244:247], v[198:201]\n"
      "v_exp_f32 v141, v141\n"
      "s_waitcnt lgkmcnt(4)\n"
      "v_mfma_f32_16x16x32_bf16 v[154:157], v[50:53], v[226:229], v[154:157]\n"
      "ds_read_b128 v[74:77], v20 offset:49152\n"
      "v_mfma_f32_16x16x32_bf16 v[170:173], v[50:53], a[216:219], v[170:173]\n"
      "v_exp_f32 v142, v142\n"
      "v_mfma_f32_16x16x32_bf16 v[186:189], v[50:53], a[248:251], v[186:189]\n"
      "v_exp_f32 v143, v143\n"
      "v_mfma_f32_16x16x32_bf16 v[158:161], v[54:57], v[226:229], v[158:161]\n"
      "ds_read_b128 v[78:81], v20 offset:57344\n"
      "v_mfma_f32_16x16x32_bf16 v[174:177], v[54:57], a[216:219], v[174:177]\n"
      "v_exp_f32 v144, v144\n"
      "v_mfma_f32_16x16x32_bf16 v[190:193], v[54:57], a[248:251], v[190:193]\n"
      "v_exp_f32 v145, v145\n"
      "s_waitcnt lgkmcnt(4)\n"
      "v_mfma_f32_16x16x32_bf16 v[162:165], v[58:61], v[226:229], v[162:165]\n"
      "ds_read_b64_tr_b16 v[50:51], v21 offset:0\n"
      "ds_read_b64_tr_b16 v[52:53], v21 offset:8192\n"
      "v_mfma_f32_16x16x32_bf16 v[178:181], v[58:61], a[216:219], v[178:181]\n"
      "v_cvt_pk_bf16_f32 v98, v138, v139\n"
      "v_cvt_pk_bf16_f32 v99, v140, v141\n"
      "v_mfma_f32_16x16x32_bf16 v[194:197], v[58:61], a[248:251], v[194:197]\n"
      "v_cvt_pk_bf16_f32 v100, v142, v143\n"
      "v_cvt_pk_bf16_f32 v101, v144, v145\n"
      "v_mfma_f32_16x16x32_bf16 v[166:169], v[62:65], v[226:229], v[166:169]\n"
      "ds_read_b64_tr_b16 v[54:55], v21 offset:16384\n"
      "ds_read_b64_tr_b16 v[56:57], v21 offset:24576\n"
      "v_mfma_f32_16x16x32_bf16 v[182:185], v[62:65], a[216:219], v[182:185]\n"
      "v_exp_f32 v146, v146\n"
      "v_mfma_f32_16x16x32_bf16 v[198:201], v[62:65], a[248:251], v[198:201]\n"
      "v_exp_f32 v147, v147\n"
      "s_waitcnt lgkmcnt(6)\n"
      "v_mfma_f32_16x16x32_bf16 v[154:157], v[66:69], v[230:233], v[154:157]\n"
      "ds_read_b64_tr_b16 v[58:59], v22 offset:0\n"
      "ds_read_b64_tr_b16 v[60:61], v22 offset:8192\n"
      "v_mfma_f32_16x16x32_bf16 v[170:173], v[66:69], a[220:223], v[170:173]\n"
      "v_exp_f32 v148, v148\n"
      "v_mfma_f32_16x16x32_bf16 v[186:189], v[66:69], a[252:255], v[186:189]\n"
      "v_exp_f32 v149, v149\n"
      "v_mfma_f32_16x16x32_bf16 v[158:161], v[70:73], v[230:233], v[158:161]\n"
      "ds_read_b64_tr_b16 v[62:63], v22 offset:16384\n"
      "ds_read_b64_tr_b16 v[64:65], v22 offset:24576\n"
      "v_mfma_f32_16x16x32_bf16 v[174:177], v[70:73], a[220:223], v[174:177]\n"
      "v_exp_f32 v150, v150\n"
      "v_mfma_f32_16x16x32_bf16 v[190:193], v[70:73], a[252:255], v[190:193]\n"
      "v_exp_f32 v151, v151\n"
      "s_waitcnt lgkmcnt(8)\n"
      "v_mfma_f32_16x16x32_bf16 v[162:165], v[74:77], v[230:233], v[162:165]\n"
      "ds_read_b64_tr_b16 v[66:67], v23 offset:0\n"
      "ds_read_b64_tr_b16 v[68:69], v23 offset:8192\n"
      "v_mfma_f32_16x16x32_bf16 v[178:181], v[74:77], a[220:223], v[178:181]\n"
      "v_exp_f32 v152, v152\n"
      "v_mfma_f32_16x16x32_bf16 v[194:197], v[74:77], a[252:255], v[194:197]\n"
      "v_exp_f32 v153, v153\n"
      "v_mfma_f32_16x16x32_bf16 v[166:169], v[78:81], v[230:233], v[166:169]\n"
      "ds_read_b64_tr_b16 v[70:71], v23 offset:16384\n"
      "ds_read_b64_tr_b16 v[72:73], v23 offset:24576\n"
      "v_mfma_f32_16x16x32_bf16 v[182:185], v[78:81], a[220:223], v[182:185]\n"
      "v_cvt_pk_bf16_f32 v102, v146, v147\n"
      "v_cvt_pk_bf16_f32 v103, v148, v149\n"
      "v_mfma_f32_16x16x32_bf16 v[198:201], v[78:81], a[252:255], v[198:201]\n"
      "v_cvt_pk_bf16_f32 v104, v150, v151\n"
      "v_cvt_pk_bf16_f32 v105, v152, v153\n"
      "v_mfma_f32_16x16x32_bf16 a[192:195], v[34:37], v[82:85], a[192:195]\n"
      "s_add_u32 m0, %4, 98304\n"
      "v_xor_b32 v10, 0, v12\n"
      "global_load_lds_dwordx4 v10, s[90:91]\n"
      "v_mfma_f32_16x16x32_bf16 a[192:195], v[34:37], v[86:89], a[192:195]\n"
      "s_add_u32 m0, m0, 1024\n"
      "v_xor_b32 v10, 1088, v12\n"
      "global_load_lds_dwordx4 v10, s[90:91]\n"
      "v_mfma_f32_16x16x32_bf16 a[196:199], v[34:37], v[90:93], a[196:199]\n"
      "s_add_u32 m0, m0, 1024\n"
      "v_xor_b32 v10, 2176, v12\n"
      "global_load_lds_dwordx4 v10, s[90:91]\n"
      "v_mfma_f32_16x16x32_bf16 a[196:199], v[34:37], v[94:97], a[196:199]\n"
      "s_add_u32 m0, m0, 1024\n"
      "v_xor_b32 v10, 3264, v12\n"
      "global_load_lds_dwordx4 v10, s[90:91]\n"
      "v_mfma_f32_16x16x32_bf16 a[200:203], v[34:37], v[98:101], a[200:203]\n"
      "s_add_u32 m0, m0, 1024\n"
      "v_xor_b32 v10, 4096, v12\n"
      "global_load_lds_dwordx4 v10, s[90:91]\n"
      "v_mfma_f32_16x16x32_bf16 a[200:203], v[34:37], v[102:105], a[200:203]\n"
      "s_add_u32 m0, m0, 1024\n"
      "v_xor_b32 v10, 5184, v12\n"
      "global_load_lds_dwordx4 v10, s[90:91]\n"
      "s_waitcnt lgkmcnt(8)\n"
      "v_mfma_f32_16x16x32_bf16 a[0:3], v[50:53], v[82:85], a[0:3]\n"
      "ds_read_b64_tr_b16 v[74:75], v24 offset:0\n"
      "v_mfma_f32_16x16x32_bf16 a[64:67], v[50:53], v[90:93], a[64:67]\n"
      "ds_read_b64_tr_b16 v[76:77], v24 offset:8192\n"
      "s_add_u32 m0, m0, 1024\n"
      "v_xor_b32 v10, 6272, v12\n"
      "global_load_lds_dwordx4 v10, s[90:91]\n"
      "v_mfma_f32_16x16x32_bf16 a[128:131], v[50:53], v[98:101], a[128:131]\n"
      "s_add_u32 m0, m0, 1024\n"
      "v_xor_b32 v10, 7360, v12\n"
      "global_load_lds_dwordx4 v10, s[90:91]\n"
      "v_max3_f32 v29, v154, v155, v156\n"
      "v_mfma_f32_16x16x32_bf16 a[0:3], v[54:57], v[86:89], a[0:3]\n"
      "ds_read_b64_tr_b16 v[78:79], v24 offset:16384\n"
      "v_mfma_f32_16x16x32_bf16 a[64:67], v[54:57], v[94:97], a[64:67]\n"
      "ds_read_b64_tr_b16 v[80:81], v24 offset:24576\n"
      "v_max3_f32 v29, v29, v157, v158\n"
      "v_mfma_f32_16x16x32_bf16 a[128:131], v[54:57], v[102:105], a[128:131]\n"
      "v_max3_f32 v29, v29, v159, v160\n"
      "v_max3_f32 v29, v29, v161, v162\n"
      "s_waitcnt lgkmcnt(8)\n"
      "v_mfma_f32_16x16x32_bf16 a[4:7], v[58:61], v[82:85], a[4:7]\n"
      "ds_read_b64_tr_b16 v[50:51], v25 offset:0\n"
      "v_mfma_f32_16x16x32_bf16 a[68:71], v[58:61], v[90:93], a[68:71]\n"
      "ds_read_b64_tr_b16 v[52:53], v25 offset:8192\n"
      "v_max3_f32 v29, v29, v163, v164\n"
      "v_mfma_f32_16x16x32_bf16 a[132:135], v[58:61], v[98:101], a[132:135]\n"
      "v_max3_f32 v29, v29, v165, v166\n"
      "v_max3_f32 v29, v29, v167, v168\n"
      "v_mfma_f32_16x16x32_bf16 a[4:7], v[62:65], v[86:89], a[4:7]\n"
      "ds_read_b64_tr_b16 v[54:55], v25 offset:16384\n"
      "v_mfma_f32_16x16x32_bf16 a[68:71], v[62:65], v[94:97], a[68:71]\n"
      "ds_read_b64_tr_b16 v[56:57], v25 offset:24576\n"
      "v_max_f32 v29, v29, v169\n"
      "v_mfma_f32_16x16x32_bf16 a[132:135], v[62:65], v[102:105], a[132:135]\n"
      "v_mov_b32 v3, v29\n"
      "s_nop 1\n"
      "v_permlane32_swap_b32 v3, v29\n"
      "s_waitcnt lgkmcnt(8)\n"
      "v_mfma_f32_16x16x32_bf16 a[8:11], v[66:69], v[82:85], a[8:11]\n"
      "ds_read_b64_tr_b16 v[58:59], v26 offset:0\n"
      "v_mfma_f32_16x16x32_bf16 a[72:75], v[66:69], v[90:93], a[72:75]\n"
      "ds_read_b64_tr_b16 v[60:61], v26 offset:8192\n"
      "v_max_f32 v29, v29, v3\n"
      "v_mfma_f32_16x16x32_bf16 a[136:139], v[66:69], v[98:101], a[136:139]\n"
      "v_mov_b32 v3, v29\n"
      "s_nop 1\n"
      "v_permlane16_swap_b32 v3, v29\n"
      "v_mfma_f32_16x16x32_bf16 a[8:11], v[70:73], v[86:89], a[8:11]\n"
      "ds_read_b64_tr_b16 v[62:63], v26 offset:16384\n"
      "v_mfma_f32_16x16x32_bf16 a[72:75], v[70:73], v[94:97], a[72:75]\n"
      "ds_read_b64_tr_b16 v[64:65], v26 offset:24576\n"
      "v_max_f32 v29, v29, v3\n"
      "v_mfma_f32_16x16x32_bf16 a[136:139], v[70:73], v[102:105], a[136:139]\n"
      "v_max3_f32 v30, v170, v171, v172\n"
      "v_max3_f32 v30, v30, v173, v174\n"
      "s_waitcnt lgkmcnt(8)\n"
      "v_mfma_f32_16x16x32_bf16 a[12:15], v[74:77], v[82:85], a[12:15]\n"
      "ds_read_b64_tr_b16 v[66:67], v27 offset:0\n"
      "v_mfma_f32_16x16x32_bf16 a[76:79], v[74:77], v[90:93], a[76:79]\n"
      "ds_read_b64_tr_b16 v[68:69], v27 offset:8192\n"
      "v_max3_f32 v30, v30, v175, v176\n"
      "v_mfma_f32_16x16x32_bf16 a[140:143], v[74:77], v[98:101], a[140:143]\n"
      "v_max3_f32 v30, v30, v177, v178\n"
      "v_max3_f32 v30, v30, v179, v180\n"
      "v_mfma_f32_16x16x32_bf16 a[12:15], v[78:81], v[86:89], a[12:15]\n"
      "ds_read_b64_tr_b16 v[70:71], v27 offset:16384\n"
      "v_mfma_f32_16x16x32_bf16 a[76:79], v[78:81], v[94:97], a[76:79]\n"
      "ds_read_b64_tr_b16 v[72:73], v27 offset:24576\n"
      "v_max3_f32 v30, v30, v181, v182\n"
      "v_mfma_f32_16x16x32_bf16 a[140:143], v[78:81], v[102:105], a[140:143]\n"
      "v_max3_f32 v30, v30, v183, v184\n"
      "v_max_f32 v30, v30, v185\n"
      "s_waitcnt lgkmcnt(8)\n"
      "v_mfma_f32_16x16x32_bf16 a[16:19], v[50:53], v[82:85], a[16:19]\n"
      "ds_read_b64_tr_b16 v[74:75], v28 offset:0\n"
      "v_mfma_f32_16x16x32_bf16 a[80:83], v[50:53], v[90:93], a[80:83]\n"
      "ds_read_b64_tr_b16 v[76:77], v28 offset:8192\n"
      "v_mov_b32 v4, v30\n"
      "v_mfma_f32_16x16x32_bf16 a[144:147], v[50:53], v[98:101], a[144:147]\n"
      "s_nop 0\n"
      "v_permlane32_swap_b32 v4, v30\n"
      "v_max_f32 v30, v30, v4\n"
      "v_mfma_f32_16x16x32_bf16 a[16:19], v[54:57], v[86:89], a[16:19]\n"
      "ds_read_b64_tr_b16 v[78:79], v28 offset:16384\n"
      "v_mfma_f32_16x16x32_bf16 a[80:83], v[54:57], v[94:97], a[80:83]\n"
      "ds_read_b64_tr_b16 v[80:81], v28 offset:24576\n"
      "v_mov_b32 v4, v30\n"
      "v_mfma_f32_16x16x32_bf16 a[144:147], v[54:57], v[102:105], a[144:147]\n"
      "s_nop 0\n"
      "v_permlane16_swap_b32 v4, v30\n"
      "v_max_f32 v30, v30, v4\n"
      "s_waitcnt lgkmcnt(8)\n"
      "v_mfma_f32_16x16x32_bf16 a[20:23], v[58:61], v[82:85], a[20:23]\n"
      "ds_read_b64_tr_b16 v[50:51], v21 offset:256\n"
      "v_mfma_f32_16x16x32_bf16 a[84:87], v[58:61], v[90:93], a[84:87]\n"
      "ds_read_b64_tr_b16 v[52:53], v21 offset:8448\n"
      "v_max3_f32 v31, v186, v187, v188\n"
      "v_mfma_f32_16x16x32_bf16 a[148:151], v[58:61], v[98:101], a[148:151]\n"
      "v_max3_f32 v31, v31, v189, v190\n"
      "v_max3_f32 v31, v31, v191, v192\n"
      "v_mfma_f32_16x16x32_bf16 a[20:23], v[62:65], v[86:89], a[20:23]\n"
      "ds_read_b64_tr_b16 v[54:55], v21 offset:16640\n"
      "v_mfma_f32_16x16x32_bf16 a[84:87], v[62:65], v[94:97], a[84:87]\n"
      "ds_read_b64_tr_b16 v[56:57], v21 offset:24832\n"
      "v_max3_f32 v31, v31, v193, v194\n"
      "v_mfma_f32_16x16x32_bf16 a[148:151], v[62:65], v[102:105], a[148:151]\n"
      "v_max3_f32 v31, v31, v195, v196\n"
      "v_max3_f32 v31, v31, v197, v198\n"
      "s_waitcnt lgkmcnt(8)\n"
      "v_mfma_f32_16x16x32_bf16 a[24:27], v[66:69], v[82:85], a[24:27]\n"
      "ds_read_b64_tr_b16 v[58:59], v22 offset:256\n"
      "v_mfma_f32_16x16x32_bf16 a[88:91], v[66:69], v[90:93], a[88:91]\n"
      "ds_read_b64_tr_b16 v[60:61], v22 offset:8448\n"
      "v_max3_f32 v31, v31, v199, v200\n"
      "v_mfma_f32_16x16x32_bf16 a[152:155], v[66:69], v[98:101], a[152:155]\n"
      "v_max_f32 v31, v31, v201\n"
      "v_mov_b32 v5, v31\n"
      "v_mfma_f32_16x16x32_bf16 a[24:27], v[70:73], v[86:89], a[24:27]\n"
      "ds_read_b64_tr_b16 v[62:63], v22 offset:16640\n"
      "v_mfma_f32_16x16x32_bf16 a[88:91], v[70:73], v[94:97], a[88:91]\n"
      "ds_read_b64_tr_b16 v[64:65], v22 offset:24832\n"
      "v_permlane32_swap_b32 v5, v31\n"
      "v_mfma_f32_16x16x32_bf16 a[152:155], v[70:73], v[102:105], a[152:155]\n"
      "v_max_f32 v31, v31, v5\n"
      "v_mov_b32 v5, v31\n"
      "s_waitcnt lgkmcnt(8)\n"
      "v_mfma_f32_16x16x32_bf16 a[28:31], v[74:77], v[82:85], a[28:31]\n"
      "ds_read_b64_tr_b16 v[66:67], v23 offset:256\n"
      "v_mfma_f32_16x16x32_bf16 a[92:95], v[74:77], v[90:93], a[92:95]\n"
      "ds_read_b64_tr_b16 v[68:69], v23 offset:8448\n"
      "v_permlane16_swap_b32 v5, v31\n"
      "v_mfma_f32_16x16x32_bf16 a[156:159], v[74:77], v[98:101], a[156:159]\n"
      "v_max_f32 v31, v31, v5\n"
      "v_mfma_f32_16x16x32_bf16 a[28:31], v[78:81], v[86:89], a[28:31]\n"
      "ds_read_b64_tr_b16 v[70:71], v23 offset:16640\n"
      "v_mfma_f32_16x16x32_bf16 a[92:95], v[78:81], v[94:97], a[92:95]\n"
      "ds_read_b64_tr_b16 v[72:73], v23 offset:24832\n"
      "v_mfma_f32_16x16x32_bf16 a[156:159], v[78:81], v[102:105], a[156:159]\n"
      "s_waitcnt lgkmcnt(8)\n"
      "v_mfma_f32_16x16x32_bf16 a[32:35], v[50:53], v[82:85], a[32:35]\n"
      "ds_read_b64_tr_b16 v[74:75], v24 offset:256\n"
      "v_mfma_f32_16x16x32_bf16 a[96:99], v[50:53], v[90:93], a[96:99]\n"
      "ds_read_b64_tr_b16 v[76:77], v24 offset:8448\n"
      "v_mfma_f32_16x16x32_bf16 a[160:163], v[50:53], v[98:101], a[160:163]\n"
      "v_mfma_f32_16x16x32_bf16 a[32:35], v[54:57], v[86:89], a[32:35]\n"
      "ds_read_b64_tr_b16 v[78:79], v24 offset:16640\n"
      "v_mfma_f32_16x16x32_bf16 a[96:99], v[54:57], v[94:97], a[96:99]\n"
      "ds_read_b64_tr_b16 v[80:81], v24 offset:24832\n"
      "v_mfma_f32_16x16x32_bf16 a[160:163], v[54:57], v[102:105], a[160:163]\n"
      "s_waitcnt lgkmcnt(8)\n"
      "v_mfma_f32_16x16x32_bf16 a[36:39], v[58:61], v[82:85], a[36:39]\n"
      "ds_read_b64_tr_b16 v[50:51], v25 offset:256\n"
      "v_mfma_f32_16x16x32_bf16 a[100:103], v[58:61], v[90:93], a[100:103]\n"
      "ds_read_b64_tr_b16 v[52:53], v25 offset:8448\n"
      "v_mfma_f32_16x16x32_bf16 a[164:167], v[58:61], v[98:101], a[164:167]\n"
      "v_mfma_f32_16x16x32_bf16 a[36:39], v[62:65], v[86:89], a[36:39]\n"
      "ds_read_b64_tr_b16 v[54:55], v25 offset:16640\n"
      "v_mfma_f32_16x16x32_bf16 a[100:103], v[62:65], v[94:97], a[100:103]\n"
      "ds_read_b64_tr_b16 v[56:57], v25 offset:24832\n"
      "v_mfma_f32_16x16x32_bf16 a[164:167], v[62:65], v[102:105], a[164:167]\n"
      "s_waitcnt lgkmcnt(8)\n"
      "v_mfma_f32_16x16x32_bf16 a[40:43], v[66:69], v[82:85], a[40:43]\n"
      "ds_read_b64_tr_b16 v[58:59], v26 offset:256\n"
      "v_mfma_f32_16x16x32_bf16 a[104:107], v[66:69], v[90:93], a[104:107]\n"
      "ds_read_b64_tr_b16 v[60:61], v26 offset:8448\n"
      "v_mfma_f32_16x16x32_bf16 a[168:171], v[66:69], v[98:101], a[168:171]\n"
      "v_mfma_f32_16x16x32_bf16 a[40:43], v[70:73], v[86:89], a[40:43]\n"
      "ds_read_b64_tr_b16 v[62:63], v26 offset:16640\n"
      "v_mfma_f32_16x16x32_bf16 a[104:107], v[70:73], v[94:97], a[104:107]\n"
      "ds_read_b64_tr_b16 v[64:65], v26 offset:24832\n"
      "v_mfma_f32_16x16x32_bf16 a[168:171], v[70:73], v[102:105], a[168:171]\n"
      "s_waitcnt lgkmcnt(8)\n"
      "v_mfma_f32_16x16x32_bf16 a[44:47], v[74:77], v[82:85], a[44:47]\n"
      "ds_read_b64_tr_b16 v[66:67], v27 offset:256\n"
      "v_mfma_f32_16x16x32_bf16 a[108:111], v[74:77], v[90:93], a[108:111]\n"
      "ds_read_b64_tr_b16 v[68:69], v27 offset:8448\n"
      "v_mfma_f32_16x16x32_bf16 a[172:175], v[74:77], v[98:101], a[172:175]\n"
      "v_mfma_f32_16x16x32_bf16 a[44:47], v[78:81], v[86:89], a[44:47]\n"
      "ds_read_b64_tr_b16 v[70:71], v27 offset:16640\n"
      "v_mfma_f32_16x16x32_bf16 a[108:111], v[78:81], v[94:97], a[108:111]\n"
      "ds_read_b64_tr_b16 v[72:73], v27 offset:24832\n"
      "v_mfma_f32_16x16x32_bf16 a[172:175], v[78:81], v[102:105], a[172:175]\n"
      "s_waitcnt lgkmcnt(8)\n"
      "v_mfma_f32_16x16x32_bf16 a[48:51], v[50:53], v[82:85], a[48:51]\n"
      "ds_read_b64_tr_b16 v[74:75], v28 offset:256\n"
      "v_mfma_f32_16x16x32_bf16 a[112:115], v[50:53], v[90:93], a[112:115]\n"
      "ds_read_b64_tr_b16 v[76:77], v28 offset:8448\n"
      "v_mfma_f32_16x16x32_bf16 a[176:179], v[50:53], v[98:101], a[176:179]\n"
      "v_mfma_f32_16x16x32_bf16 a[48:51], v[54:57], v[86:89], a[48:51]\n"
      "ds_read_b64_tr_b16 v[78:79], v28 offset:16640\n"
      "v_mfma_f32_16x16x32_bf16 a[112:115], v[54:57], v[94:97], a[112:115]\n"
      "ds_read_b64_tr_b16 v[80:81], v28 offset:24832\n"
      "v_mfma_f32_16x16x32_bf16 a[176:179], v[54:57], v[102:105], a[176:179]\n"
      "s_waitcnt lgkmcnt(8)\n"
      "v_mfma_f32_16x16x32_bf16 a[52:55], v[58:61], v[82:85], a[52:55]\n"
      "v_mfma_f32_16x16x32_bf16 a[116:119], v[58:61], v[90:93], a[116:119]\n"
      "v_mfma_f32_16x16x32_bf16 a[180:183], v[58:61], v[98:101], a[180:183]\n"
      "v_mfma_f32_16x16x32_bf16 a[52:55], v[62:65], v[86:89], a[52:55]\n"
      "v_mfma_f32_16x16x32_bf16 a[116:119], v[62:65], v[94:97], a[116:119]\n"
      "v_mfma_f32_16x16x32_bf16 a[180:183], v[62:65], v[102:105], a[180:183]\n"
      "s_waitcnt lgkmcnt(4)\n"
      "v_mfma_f32_16x16x32_bf16 a[56:59], v[66:69], v[82:85], a[56:59]\n"
      "v_mfma_f32_16x16x32_bf16 a[120:123], v[66:69], v[90:93], a[120:123]\n"
      "v_mfma_f32_16x16x32_bf16 a[184:187], v[66:69], v[98:101], a[184:187]\n"
      "v_mfma_f32_16x16x32_bf16 a[56:59], v[70:73], v[86:89], a[56:59]\n"
      "v_mfma_f32_16x16x32_bf16 a[120:123], v[70:73], v[94:97], a[120:123]\n"
      "v_mfma_f32_16x16x32_bf16 a[184:187], v[70:73], v[102:105], a[184:187]\n"
      "s_waitcnt lgkmcnt(2)\n"
      "v_mfma_f32_16x16x32_bf16 a[60:63], v[74:77], v[82:85], a[60:63]\n"
      "v_mfma_f32_16x16x32_bf16 a[124:127], v[74:77], v[90:93], a[124:127]\n"
      "v_mfma_f32_16x16x32_bf16 a[188:191], v[74:77], v[98:101], a[188:191]\n"
      "s_waitcnt lgkmcnt(0)\n"
      "v_mfma_f32_16x16x32_bf16 a[60:63], v[78:81], v[86:89], a[60:63]\n"
      "v_mfma_f32_16x16x32_bf16 a[124:127], v[78:81], v[94:97], a[124:127]\n"
      "v_mfma_f32_16x16x32_bf16 a[188:191], v[78:81], v[102:105], a[188:191]\n"
      "s_add_u32 s88, s88, 32768\n"
      "s_addc_u32 s89, s89, 0\n"
      "s_add_u32 s90, s90, 32768\n"
      "s_addc_u32 s91, s91, 0\n"
      "v_max3_f32 v3, v29, v30, v31\n"
      "v_cmp_lt_f32 vcc, 0x41000000, v3\n"
      "s_nop 1\n"
      "s_cbranch_vccnz SLOW1_%=\n"
      "BACK1_%=:\n"
      "s_waitcnt vmcnt(0) lgkmcnt(0)\n"
      "s_barrier\n"
      "ds_read_b128 v[50:53], v13 offset:0\n"
      "ds_read_b128 v[54:57], v13 offset:8192\n"
      "ds_read_b128 v[58:61], v13 offset:16384\n"
      "ds_read_b128 v[62:65], v13 offset:24576\n"
      "ds_read_b128 v[66:69], v14 offset:0\n"
      "ds_read_b128 v[70:73], v14 offset:8192\n"
      "s_waitcnt lgkmcnt(4)\n"
      "v_mfma_f32_16x16x32_bf16 v[106:109], v[50:53], v[202:205], v[38:41]\n"
      "ds_read_b128 v[74:77], v14 offset:16384\n"
      "v_mfma_f32_16x16x32_bf16 v[122:125], v[50:53], v[234:237], v[42:45]\n"
      "s_add_u32 m0, %4, 32768\n"
      "v_xor_b32 v10, 0, v11\n"
      "global_load_lds_dwordx4 v10, s[88:89]\n"
      "s_add_u32 m0, m0, 1024\n"
      "v_xor_b32 v10, 1056, v11\n"
      "global_load_lds_dwordx4 v10, s[88:89]\n"
      "v_mfma_f32_16x16x32_bf16 v[138:141], v[50:53], a[224:227], v[46:49]\n"
      "s_add_u32 m0, m0, 1024\n"
      "v_xor_b32 v10, 2112, v11\n"
      "global_load_lds_dwordx4 v10, s[88:89]\n"
      "s_add_u32 m0, m0, 1024\n"
      "v_xor_b32 v10, 3168, v11\n"
      "global_load_lds_dwordx4 v10, s[88:89]\n"
      "v_mfma_f32_16x16x32_bf16 v[110:113], v[54:57], v[202:205], v[38:41]\n"
      "ds_read_b128 v[78:81], v14 offset:24576\n"
      "v_mfma_f32_16x16x32_bf16 v[126:129], v[54:57], v[234:237], v[42:45]\n"
      "s_add_u32 m0, m0, 1024\n"
      "v_xor_b32 v10, 4224, v11\n"
      "global_load_lds_dwordx4 v10, s[88:89]\n"
      "s_add_u32 m0, m0, 1024\n"
      "v_xor_b32 v10, 5280, v11\n"
      "global_load_lds_dwordx4 v10, s[88:89]\n"
      "v_mfma_f32_16x16x32_bf16 v[142:145], v[54:57], a[224:227], v[46:49]\n"
      "s_add_u32 m0, m0, 1024\n"
      "v_xor_b32 v10, 6336, v11\n"
      "global_load_lds_dwordx4 v10, s[88:89]\n"
      "s_add_u32 m0, m0, 1024\n"
      "v_xor_b32 v10, 7392, v11\n"
      "global_load_lds_dwordx4 v10, s[88:89]\n"
      "s_waitcnt lgkmcnt(4)\n"
      "v_mfma_f32_16x16x32_bf16 v[114:117], v[58:61], v[202:205], v[38:41]\n"
      "ds_read_b128 v[50:53], v15 offset:0\n"
      "v_mfma_f32_16x16x32_bf16 v[130:133], v[58:61], v[234:237], v[42:45]\n"
      "v_exp_f32 v154, v154\n"
      "v_mfma_f32_16x16x32_bf16 v[146:149], v[58:61], a[224:227], v[46:49]\n"
      "v_exp_f32 v155, v155\n"
      "v_mfma_f32_16x16x32_bf16 v[118:121], v[62:65], v[202:205], v[38:41]\n"
      "ds_read_b128 v[54:57], v15 offset:8192\n"
      "v_mfma_f32_16x16x32_bf16 v[134:137], v[62:65], v[234:237], v[42:45]\n"
      "v_exp_f32 v156, v156\n"
      "v_mfma_f32_16x16x32_bf16 v[150:153], v[62:65], a[224:227], v[46:49]\n"
      "v_exp_f32 v157, v157\n"
      "s_waitcnt lgkmcnt(4)\n"
      "v_mfma_f32_16x16x32_bf16 v[106:109], v[66:69], v[206:209], v[106:109]\n"
      "ds_read_b128 v[58:61], v15 offset:16384\n"
      "v_mfma_f32_16x16x32_bf16 v[122:125], v[66:69], v[238:241], v[122:125]\n"
      "v_exp_f32 v158, v158\n"
      "v_mfma_f32_16x16x32_bf16 v[138:141], v[66:69], a[228:231], v[138:141]\n"
      "v_exp_f32 v159, v159\n"
      "v_mfma_f32_16x16x32_bf16 v[110:113], v[70:73], v[206:209], v[110:113]\n"
      "ds_read_b128 v[62:65], v15 offset:24576\n"
      "v_mfma_f32_16x16x32_bf16 v[126:129], v[70:73], v[238:241], v[126:129]\n"
      "v_exp_f32 v160, v160\n"
      "v_mfma_f32_16x16x32_bf16 v[142:145], v[70:73], a[228:231], v[142:145]\n"
      "v_exp_f32 v161, v161\n"
      "s_waitcnt lgkmcnt(4)\n"
      "v_mfma_f32_16x16x32_bf16 v[114:117], v[74:77], v[206:209], v[114:117]\n"
      "ds_read_b128 v[66:69], v16 offset:0\n"
      "v_mfma_f32_16x16x32_bf16 v[130:133], v[74:77], v[238:241], v[130:133]\n"
      "v_cvt_pk_bf16_f32 v82, v154, v155\n"
      "v_cvt_pk_bf16_f32 v83, v156, v157\n"
      "v_mfma_f32_16x16x32_bf16 v[146:149], v[74:77], a[228:231], v[146:149]\n"
      "v_cvt_pk_bf16_f32 v84, v158, v159\n"
      "v_cvt_pk_bf16_f32 v85, v160, v161\n"
      "v_mfma_f32_16x16x32_bf16 v[118:121], v[78:81], v[206:209], v[118:121]\n"
      "ds_read_b128 v[70:73], v16 offset:8192\n"
      "v_mfma_f32_16x16x32_bf16 v[134:137], v[78:81], v[238:241], v[134:137]\n"
      "v_exp_f32 v162, v162\n"
      "v_mfma_f32_16x16x32_bf16 v[150:153], v[78:81], a[228:231], v[150:153]\n"
      "v_exp_f32 v163, v163\n"
      "s_waitcnt lgkmcnt(4)\n"
      "v_mfma_f32_16x16x32_bf16 v[106:109], v[50:53], v[210:213], v[106:109]\n"
      "ds_read_b128 v[74:77], v16 offset:16384\n"
      "v_mfma_f32_16x16x32_bf16 v[122:125], v[50:53], v[242:245], v[122:125]\n"
      "v_exp_f32 v164, v164\n"
      "v_mfma_f32_16x16x32_bf16 v[138:141], v[50:53], a[232:235], v[138:141]\n"
      "v_exp_f32 v165, v165\n"
      "v_mfma_f32_16x16x32_bf16 v[110:113], v[54:57], v[210:213], v[110:113]\n"
      "ds_read_b128 v[78:81], v16 offset:24576\n"
      "v_mfma_f32_16x16x32_bf16 v[126:129], v[54:57], v[242:245], v[126:129]\n"
      "v_exp_f32 v166, v166\n"
      "v_mfma_f32_16x16x32_bf16 v[142:145], v[54:57], a[232:235], v[142:145]\n"
      "v_exp_f32 v167, v167\n"
      "s_waitcnt lgkmcnt(4)\n"
      "v_mfma_f32_16x16x32_bf16 v[114:117], v[58:61], v[210:213], v[114:117]\n"
      "ds_read_b128 v[50:53], v17 offset:0\n"
      "v_mfma_f32_16x16x32_bf16 v[130:133], v[58:61], v[242:245], v[130:133]\n"
      "v_exp_f32 v168, v168\n"
      "v_mfma_f32_16x16x32_bf16 v[146:149], v[58:61], a[232:235], v[146:149]\n"
      "v_exp_f32 v169, v169\n"
      "v_mfma_f32_16x16x32_bf16 v[118:121], v[62:65], v[210:213], v[118:121]\n"
      "ds_read_b128 v[54:57], v17 offset:8192\n"
      "v_mfma_f32_16x16x32_bf16 v[134:137], v[62:65], v[242:245], v[134:137]\n"
      "v_cvt_pk_bf16_f32 v86, v162, v163\n"
      "v_cvt_pk_bf16_f32 v87, v164, v165\n"
      "v_mfma_f32_16x16x32_bf16 v[150:153], v[62:65], a[232:235], v[150:153]\n"
      "v_cvt_pk_bf16_f32 v88, v166, v167\n"
      "v_cvt_pk_bf16_f32 v89, v168, v169\n"
      "s_waitcnt lgkmcnt(4)\n"
      "v_mfma_f32_16x16x32_bf16 v[106:109], v[66:69], v[214:217], v[106:109]\n"
      "ds_read_b128 v[58:61], v17 offset:16384\n"
      "v_mfma_f32_16x16x32_bf16 v[122:125], v[66:69], a[204:207], v[122:125]\n"
      "v_exp_f32 v170, v170\n"
      "v_mfma_f32_16x16x32_bf16 v[138:141], v[66:69], a[236:239], v[138:141]\n"
      "v_exp_f32 v171, v171\n"
      "v_mfma_f32_16x16x32_bf16 v[110:113], v[70:73], v[214:217], v[110:113]\n"
      "ds_read_b128 v[62:65], v17 offset:24576\n"
      "v_mfma_f32_16x16x32_bf16 v[126:129], v[70:73], a[204:207], v[126:129]\n"
      "v_exp_f32 v172, v172\n"
      "v_mfma_f32_16x16x32_bf16 v[142:145], v[70:73], a[236:239], v[142:145]\n"
      "v_exp_f32 v173, v173\n"
      "s_waitcnt lgkmcnt(4)\n"
      "v_mfma_f32_16x16x32_bf16 v[114:117], v[74:77], v[214:217], v[114:117]\n"
      "ds_read_b128 v[66:69], v18 offset:0\n"
      "v_mfma_f32_16x16x32_bf16 v[130:133], v[74:77], a[204:207], v[130:133]\n"
      "v_exp_f32 v174, v174\n"
      "v_mfma_f32_16x16x32_bf16 v[146:149], v[74:77], a[236:239], v[146:149]\n"
      "v_exp_f32 v175, v175\n"
      "v_mfma_f32_16x16x32_bf16 v[118:121], v[78:81], v[214:217], v[118:121]\n"
      "ds_read_b128 v[70:73], v18 offset:8192\n"
      "v_mfma_f32_16x16x32_bf16 v[134:137], v[78:81], a[204:207], v[134:137]\n"
      "v_exp_f32 v176, v176\n"
      "v_mfma_f32_16x16x32_bf16 v[150:153], v[78:81], a[236:239], v[150:153]\n"
      "v_exp_f32 v177, v177\n"
      "s_waitcnt lgkmcnt(4)\n"
      "v_mfma_f32_16x16x32_bf16 v[106:109], v[50:53], v[218:221], v[106:109]\n"
      "ds_read_b128 v[74:77], v18 offset:16384\n"
      "v_mfma_f32_16x16x32_bf16 v[122:125], v[50:53], a[208:211], v[122:125]\n"
      "v_cvt_pk_bf16_f32 v90, v170, v171\n"
      "v_cvt_pk_bf16_f32 v91, v172, v173\n"
      "v_mfma_f32_16x16x32_bf16 v[138:141], v[50:53], a[240:243], v[138:141]\n"
      "v_cvt_pk_bf16_f32 v92, v174, v175\n"
      "v_cvt_pk_bf16_f32 v93, v176, v177\n"
      "v_mfma_f32_16x16x32_bf16 v[110:113], v[54:57], v[218:221], v[110:113]\n"
      "ds_read_b128 v[78:81], v18 offset:24576\n"
      "v_mfma_f32_16x16x32_bf16 v[126:129], v[54:57], a[208:211], v[126:129]\n"
      "v_exp_f32 v178, v178\n"
      "v_mfma_f32_16x16x32_bf16 v[142:145], v[54:57], a[240:243], v[142:145]\n"
      "v_exp_f32 v179, v179\n"
      "s_waitcnt lgkmcnt(4)\n"
      "v_mfma_f32_16x16x32_bf16 v[114:117], v[58:61], v[218:221], v[114:117]\n"
      "ds_read_b128 v[50:53], v19 offset:0\n"
      "v_mfma_f32_16x16x32_bf16 v[130:133], v[58:61], a[208:211], v[130:133]\n"
      "v_exp_f32 v180, v180\n"
      "v_mfma_f32_16x16x32_bf16 v[146:149], v[58:61], a[240:243], v[146:149]\n"
      "v_exp_f32 v181, v181\n"
      "v_mfma_f32_16x16x32_bf16 v[118:121], v[62:65], v[218:221], v[118:121]\n"
      "ds_read_b128 v[54:57], v19 offset:8192\n"
      "v_mfma_f32_16x16x32_bf16 v[134:137], v[62:65], a[208:211], v[134:137]\n"
      "v_exp_f32 v182, v182\n"
      "v_mfma_f32_16x16x32_bf16 v[150:153], v[62:65], a[240:243], v[150:153]\n"
      "v_exp_f32 v183, v183\n"
      "s_waitcnt lgkmcnt(4)\n"
      "v_mfma_f32_16x16x32_bf16 v[106:109], v[66:69], v[222:225], v[106:109]\n"
      "ds_read_b128 v[58:61], v19 offset:16384\n"
      "v_mfma_f32_16x16x32_bf16 v[122:125], v[66:69], a[212:215], v[122:125]\n"
      "v_exp_f32 v184, v184\n"
      "v_mfma_f32_16x16x32_bf16 v[138:141], v[66:69], a[244:247], v[138:141]\n"
      "v_exp_f32 v185, v185\n"
      "v_mfma_f32_16x16x32_bf16 v[110:113], v[70:73], v[222:225], v[110:113]\n"
      "ds_read_b128 v[62:65], v19 offset:24576\n"
      "v_mfma_f32_16x16x32_bf16 v[126:129], v[70:73], a[212:215], v[126:129]\n"
      "v_cvt_pk_bf16_f32 v94, v178, v179\n"
      "v_cvt_pk_bf16_f32 v95, v180, v181\n"
      "v_mfma_f32_16x16x32_bf16 v[142:145], v[70:73], a[244:247], v[142:145]\n"
      "v_cvt_pk_bf16_f32 v96, v182, v183\n"
      "v_cvt_pk_bf16_f32 v97, v184, v185\n"
      "s_waitcnt lgkmcnt(4)\n"
      "v_mfma_f32_16x16x32_bf16 v[114:117], v[74:77], v[222:225], v[114:117]\n"
      "ds_read_b128 v[66:69], v20 offset:0\n"
      "v_mfma_f32_16x16x32_bf16 v[130:133], v[74:77], a[212:215], v[130:133]\n"
      "v_exp_f32 v186, v186\n"
      "v_mfma_f32_16x16x32_bf16 v[146:149], v[74:77], a[244:247], v[146:149]\n"
      "v_exp_f32 v187, v187\n"
      "v_mfma_f32_16x16x32_bf16 v[118:121], v[78:81], v[222:225], v[118:121]\n"
      "ds_read_b128 v[70:73], v20 offset:8192\n"
      "v_mfma_f32_16x16x32_bf16 v[134:137], v[78:81], a[212:215], v[134:137]\n"
      "v_exp_f32 v188, v188\n"
      "v_mfma_f32_16x16x32_bf16 v[150:153], v[78:81], a[244:247], v[150:153]\n"
      "v_exp_f32 v189, v189\n"
      "s_waitcnt lgkmcnt(4)\n"
      "v_mfma_f32_16x16x32_bf16 v[106:109], v[50:53], v[226:229], v[106:109]\n"
      "ds_read_b128 v[74:77], v20 offset:16384\n"
      "v_mfma_f32_16x16x32_bf16 v[122:125], v[50:53], a[216:219], v[122:125]\n"
      "v_exp_f32 v190, v190\n"
      "v_mfma_f32_16x16x32_bf16 v[138:141], v[50:53], a[248:251], v[138:141]\n"
      "v_exp_f32 v191, v191\n"
      "v_mfma_f32_16x16x32_bf16 v[110:113], v[54:57], v[226:229], v[110:113]\n"
      "ds_read_b128 v[78:81], v20 offset:24576\n"
      "v_mfma_f32_16x16x32_bf16 v[126:129], v[54:57], a[216:219], v[126:129]\n"
      "v_exp_f32 v192, v192\n"
      "v_mfma_f32_16x16x32_bf16 v[142:145], v[54:57], a[248:251], v[142:145]\n"
      "v_exp_f32 v193, v193\n"
      "s_waitcnt lgkmcnt(4)\n"
      "v_mfma_f32_16x16x32_bf16 v[114:117], v[58:61], v[226:229], v[114:117]\n"
      "ds_read_b64_tr_b16 v[50:51], v21 offset:32768\n"
      "ds_read_b64_tr_b16 v[52:53], v21 offset:40960\n"
      "v_mfma_f32_16x16x32_bf16 v[130:133], v[58:61], a[216:219], v[130:133]\n"
      "v_cvt_pk_bf16_f32 v98, v186, v187\n"
      "v_cvt_pk_bf16_f32 v99, v188, v189\n"
      "v_mfma_f32_16x16x32_bf16 v[146:149], v[58:61], a[248:251], v[146:149]\n"
      "v_cvt_pk_bf16_f32 v100, v190, v191\n"
      "v_cvt_pk_bf16_f32 v101, v192, v193\n"
      "v_mfma_f32_16x16x32_bf16 v[118:121], v[62:65], v[226:229], v[118:121]\n"
      "ds_read_b64_tr_b16 v[54:55], v21 offset:49152\n"
      "ds_read_b64_tr_b16 v[56:57], v21 offset:57344\n"
      "v_mfma_f32_16x16x32_bf16 v[134:137], v[62:65], a[216:219], v[134:137]\n"
      "v_exp_f32 v194, v194\n"
      "v_mfma_f32_16x16x32_bf16 v[150:153], v[62:65], a[248:251], v[150:153]\n"
      "v_exp_f32 v195, v195\n"
      "s_waitcnt lgkmcnt(6)\n"
      "v_mfma_f32_16x16x32_bf16 v[106:109], v[66:69], v[230:233], v[106:109]\n"
      "ds_read_b64_tr_b16 v[58:59], v22 offset:32768\n"
      "ds_read_b64_tr_b16 v[60:61], v22 offset:40960\n"
      "v_mfma_f32_16x16x32_bf16 v[122:125], v[66:69], a[220:223], v[122:125]\n"
      "v_exp_f32 v196, v196\n"
      "v_mfma_f32_16x16x32_bf16 v[138:141], v[66:69], a[252:255], v[138:141]\n"
      "v_exp_f32 v197, v197\n"
      "v_mfma_f32_16x16x32_bf16 v[110:113], v[70:73], v[230:233], v[110:113]\n"
      "ds_read_b64_tr_b16 v[62:63], v22 offset:49152\n"
      "ds_read_b64_tr_b16 v[64:65], v22 offset:57344\n"
      "v_mfma_f32_16x16x32_bf16 v[126:129], v[70:73], a[220:223], v[126:129]\n"
      "v_exp_f32 v198, v198\n"
      "v_mfma_f32_16x16x32_bf16 v[142:145], v[70:73], a[252:255], v[142:145]\n"
      "v_exp_f32 v199, v199\n"
      "s_waitcnt lgkmcnt(8)\n"
      "v_mfma_f32_16x16x32_bf16 v[114:117], v[74:77], v[230:233], v[114:117]\n"
      "ds_read_b64_tr_b16 v[66:67], v23 offset:32768\n"
      "ds_read_b64_tr_b16 v[68:69], v23 offset:40960\n"
      "v_mfma_f32_16x16x32_bf16 v[130:133], v[74:77], a[220:223], v[130:133]\n"
      "v_exp_f32 v200, v200\n"
      "v_mfma_f32_16x16x32_bf16 v[146:149], v[74:77], a[252:255], v[146:149]\n"
      "v_exp_f32 v201, v201\n"
      "v_mfma_f32_16x16x32_bf16 v[118:121], v[78:81], v[230:233], v[118:121]\n"
      "ds_read_b64_tr_b16 v[70:71], v23 offset:49152\n"
      "ds_read_b64_tr_b16 v[72:73], v23 offset:57344\n"
      "v_mfma_f32_16x16x32_bf16 v[134:137], v[78:81], a[220:223], v[134:137]\n"
      "v_cvt_pk_bf16_f32 v102, v194, v195\n"
      "v_cvt_pk_bf16_f32 v103, v196, v197\n"
      "v_mfma_f32_16x16x32_bf16 v[150:153], v[78:81], a[252:255], v[150:153]\n"
      "v_cvt_pk_bf16_f32 v104, v198, v199\n"
      "v_cvt_pk_bf16_f32 v105, v200, v201\n"
      "v_mfma_f32_16x16x32_bf16 a[192:195], v[34:37], v[82:85], a[192:195]\n"
      "s_add_u32 m0, %4, 65536\n"
      "v_xor_b32 v10, 0, v12\n"
      "global_load_lds_dwordx4 v10, s[90:91]\n"
      "v_mfma_f32_16x16x32_bf16 a[192:195], v[34:37], v[86:89], a[192:195]\n"
      "s_add_u32 m0, m0, 1024\n"
      "v_xor_b32 v10, 1088, v12\n"
      "global_load_lds_dwordx4 v10, s[90:91]\n"
      "v_mfma_f32_16x16x32_bf16 a[196:199], v[34:37], v[90:93], a[196:199]\n"
      "s_add_u32 m0, m0, 1024\n"
      "v_xor_b32 v10, 2176, v12\n"
      "global_load_lds_dwordx4 v10, s[90:91]\n"
      "v_mfma_f32_16x16x32_bf16 a[196:199], v[34:37], v[94:97], a[196:199]\n"
      "s_add_u32 m0, m0, 1024\n"
      "v_xor_b32 v10, 3264, v12\n"
      "global_load_lds_dwordx4 v10, s[90:91]\n"
      "v_mfma_f32_16x16x32_bf16 a[200:203], v[34:37], v[98:101], a[200:203]\n"
      "s_add_u32 m0, m0, 1024\n"
      "v_xor_b32 v10, 4096, v12\n"
      "global_load_lds_dwordx4 v10, s[90:91]\n"
      "v_mfma_f32_16x16x32_bf16 a[200:203], v[34:37], v[102:105], a[200:203]\n"
      "s_add_u32 m0, m0, 1024\n"
      "v_xor_b32 v10, 5184, v12\n"
      "global_load_lds_dwordx4 v10, s[90:91]\n"
      "s_waitcnt lgkmcnt(8)\n"
      "v_mfma_f32_16x16x32_bf16 a[0:3], v[50:53], v[82:85], a[0:3]\n"
      "ds_read_b64_tr_b16 v[74:75], v24 offset:32768\n"
      "v_mfma_f32_16x16x32_bf16 a[64:67], v[50:53], v[90:93], a[64:67]\n"
      "ds_read_b64_tr_b16 v[76:77], v24 offset:40960\n"
      "s_add_u32 m0, m0, 1024\n"
      "v_xor_b32 v10, 6272, v12\n"
      "global_load_lds_dwordx4 v10, s[90:91]\n"
      "v_mfma_f32_16x16x32_bf16 a[128:131], v[50:53], v[98:101], a[128:131]\n"
      "s_add_u32 m0, m0, 1024\n"
      "v_xor_b32 v10, 7360, v12\n"
      "global_load_lds_dwordx4 v10, s[90:91]\n"
      "v_max3_f32 v29, v106, v107, v108\n"
      "v_mfma_f32_16x16x32_bf16 a[0:3], v[54:57], v[86:89], a[0:3]\n"
      "ds_read_b64_tr_b16 v[78:79], v24 offset:49152\n"
      "v_mfma_f32_16x16x32_bf16 a[64:67], v[54:57], v[94:97], a[64:67]\n"
      "ds_read_b64_tr_b16 v[80:81], v24 offset:57344\n"
      "v_max3_f32 v29, v29, v109, v110\n"
      "v_mfma_f32_16x16x32_bf16 a[128:131], v[54:57], v[102:105], a[128:131]\n"
      "v_max3_f32 v29, v29, v111, v112\n"
      "v_max3_f32 v29, v29, v113, v114\n"
      "s_waitcnt lgkmcnt(8)\n"
      "v_mfma_f32_16x16x32_bf16 a[4:7], v[58:61], v[82:85], a[4:7]\n"
      "ds_read_b64_tr_b16 v[50:51], v25 offset:32768\n"
      "v_mfma_f32_16x16x32_bf16 a[68:71], v[58:61], v[90:93], a[68:71]\n"
      "ds_read_b64_tr_b16 v[52:53], v25 offset:40960\n"
      "v_max3_f32 v29, v29, v115, v116\n"
      "v_mfma_f32_16x16x32_bf16 a[132:135], v[58:61], v[98:101], a[132:135]\n"
      "v_max3_f32 v29, v29, v117, v118\n"
      "v_max3_f32 v29, v29, v119, v120\n"
      "v_mfma_f32_16x16x32_bf16 a[4:7], v[62:65], v[86:89], a[4:7]\n"
      "ds_read_b64_tr_b16 v[54:55], v25 offset:49152\n"
      "v_mfma_f32_16x16x32_bf16 a[68:71], v[62:65], v[94:97], a[68:71]\n"
      "ds_read_b64_tr_b16 v[56:57], v25 offset:57344\n"
      "v_max_f32 v29, v29, v121\n"
      "v_mfma_f32_16x16x32_bf16 a[132:135], v[62:65], v[102:105], a[132:135]\n"
      "v_mov_b32 v3, v29\n"
      "s_nop 1\n"
      "v_permlane32_swap_b32 v3, v29\n"
      "s_waitcnt lgkmcnt(8)\n"
      "v_mfma_f32_16x16x32_bf16 a[8:11], v[66:69], v[82:85], a[8:11]\n"
      "ds_read_b64_tr_b16 v[58:59], v26 offset:32768\n"
      "v_mfma_f32_16x16x32_bf16 a[72:75], v[66:69], v[90:93], a[72:75]\n"
      "ds_read_b64_tr_b16 v[60:61], v26 offset:40960\n"
      "v_max_f32 v29, v29, v3\n"
      "v_mfma_f32_16x16x32_bf16 a[136:139], v[66:69], v[98:101], a[136:139]\n"
      "v_mov_b32 v3, v29\n"
      "s_nop 1\n"
      "v_permlane16_swap_b32 v3, v29\n"
      "v_mfma_f32_16x16x32_bf16 a[8:11], v[70:73], v[86:89], a[8:11]\n"
      "ds_read_b64_tr_b16 v[62:63], v26 offset:49152\n"
      "v_mfma_f32_16x16x32_bf16 a[72:75], v[70:73], v[94:97], a[72:75]\n"
      "ds_read_b64_tr_b16 v[64:65], v26 offset:57344\n"
      "v_max_f32 v29, v29, v3\n"
      "v_mfma_f32_16x16x32_bf16 a[136:139], v[70:73], v[102:105], a[136:139]\n"
      "v_max3_f32 v30, v122, v123, v124\n"
      "v_max3_f32 v30, v30, v125, v126\n"
      "s_waitcnt lgkmcnt(8)\n"
      "v_mfma_f32_16x16x32_bf16 a[12:15], v[74:77], v[82:85], a[12:15]\n"
      "ds_read_b64_tr_b16 v[66:67], v27 offset:32768\n"
      "v_mfma_f32_16x16x32_bf16 a[76:79], v[74:77], v[90:93], a[76:79]\n"
      "ds_read_b64_tr_b16 v[68:69], v27 offset:40960\n"
      "v_max3_f32 v30, v30, v127, v128\n"
      "v_mfma_f32_16x16x32_bf16 a[140:143], v[74:77], v[98:101], a[140:143]\n"
      "v_max3_f32 v30, v30, v129, v130\n"
      "v_max3_f32 v30, v30, v131, v132\n"
      "v_mfma_f32_16x16x32_bf16 a[12:15], v[78:81], v[86:89], a[12:15]\n"
      "ds_read_b64_tr_b16 v[70:71], v27 offset:49152\n"
      "v_mfma_f32_16x16x32_bf16 a[76:79], v[78:81], v[94:97], a[76:79]\n"
      "ds_read_b64_tr_b16 v[72:73], v27 offset:57344\n"
      "v_max3_f32 v30, v30, v133, v134\n"
      "v_mfma_f32_16x16x32_bf16 a[140:143], v[78:81], v[102:105], a[140:143]\n"
      "v_max3_f32 v30, v30, v135, v136\n"
      "v_max_f32 v30, v30, v137\n"
      "s_waitcnt lgkmcnt(8)\n"
      "v_mfma_f32_16x16x32_bf16 a[16:19], v[50:53], v[82:85], a[16:19]\n"
      "ds_read_b64_tr_b16 v[74:75], v28 offset:32768\n"
      "v_mfma_f32_16x16x32_bf16 a[80:83], v[50:53], v[90:93], a[80:83]\n"
      "ds_read_b64_tr_b16 v[76:77], v28 offset:40960\n"
      "v_mov_b32 v4, v30\n"
      "v_mfma_f32_16x16x32_bf16 a[144:147], v[50:53], v[98:101], a[144:147]\n"
      "s_nop 0\n"
      "v_permlane32_swap_b32 v4, v30\n"
      "v_max_f32 v30, v30, v4\n"
      "v_mfma_f32_16x16x32_bf16 a[16:19], v[54:57], v[86:89], a[16:19]\n"
      "ds_read_b64_tr_b16 v[78:79], v28 offset:49152\n"
      "v_mfma_f32_16x16x32_bf16 a[80:83], v[54:57], v[94:97], a[80:83]\n"
      "ds_read_b64_tr_b16 v[80:81], v28 offset:57344\n"
      "v_mov_b32 v4, v30\n"
      "v_mfma_f32_16x16x32_bf16 a[144:147], v[54:57], v[102:105], a[144:147]\n"
      "s_nop 0\n"
      "v_permlane16_swap_b32 v4, v30\n"
      "v_max_f32 v30, v30, v4\n"
      "s_waitcnt lgkmcnt(8)\n"
      "v_mfma_f32_16x16x32_bf16 a[20:23], v[58:61], v[82:85], a[20:23]\n"
      "ds_read_b64_tr_b16 v[50:51], v21 offset:33024\n"
      "v_mfma_f32_16x16x32_bf16 a[84:87], v[58:61], v[90:93], a[84:87]\n"
      "ds_read_b64_tr_b16 v[52:53], v21 offset:41216\n"
      "v_max3_f32 v31, v138, v139, v140\n"
      "v_mfma_f32_16x16x32_bf16 a[148:151], v[58:61], v[98:101], a[148:151]\n"
      "v_max3_f32 v31, v31, v141, v142\n"
      "v_max3_f32 v31, v31, v143, v144\n"
      "v_mfma_f32_16x16x32_bf16 a[20:23], v[62:65], v[86:89], a[20:23]\n"
      "ds_read_b64_tr_b16 v[54:55], v21 offset:49408\n"
      "v_mfma_f32_16x16x32_bf16 a[84:87], v[62:65], v[94:97], a[84:87]\n"
      "ds_read_b64_tr_b16 v[56:57], v21 offset:57600\n"
      "v_max3_f32 v31, v31, v145, v146\n"
      "v_mfma_f32_16x16x32_bf16 a[148:151], v[62:65], v[102:105], a[148:151]\n"
      "v_max3_f32 v31, v31, v147, v148\n"
      "v_max3_f32 v31, v31, v149, v150\n"
      "s_waitcnt lgkmcnt(8)\n"
      "v_mfma_f32_16x16x32_bf16 a[24:27], v[66:69], v[82:85], a[24:27]\n"
      "ds_read_b64_tr_b16 v[58:59], v22 offset:33024\n"
      "v_mfma_f32_16x16x32_bf16 a[88:91], v[66:69], v[90:93], a[88:91]\n"
      "ds_read_b64_tr_b16 v[60:61], v22 offset:41216\n"
      "v_max3_f32 v31, v31, v151, v152\n"
      "v_mfma_f32_16x16x32_bf16 a[152:155], v[66:69], v[98:101], a[152:155]\n"
      "v_max_f32 v31, v31, v153\n"
      "v_mov_b32 v5, v31\n"
      "v_mfma_f32_16x16x32_bf16 a[24:27], v[70:73], v[86:89], a[24:27]\n"
      "ds_read_b64_tr_b16 v[62:63], v22 offset:49408\n"
      "v_mfma_f32_16x16x32_bf16 a[88:91], v[70:73], v[94:97], a[88:91]\n"
      "ds_read_b64_tr_b16 v[64:65], v22 offset:57600\n"
      "v_permlane32_swap_b32 v5, v31\n"
      "v_mfma_f32_16x16x32_bf16 a[152:155], v[70:73], v[102:105], a[152:155]\n"
      "v_max_f32 v31, v31, v5\n"
      "v_mov_b32 v5, v31\n"
      "s_waitcnt lgkmcnt(8)\n"
      "v_mfma_f32_16x16x32_bf16 a[28:31], v[74:77], v[82:85], a[28:31]\n"
      "ds_read_b64_tr_b16 v[66:67], v23 offset:33024\n"
      "v_mfma_f32_16x16x32_bf16 a[92:95], v[74:77], v[90:93], a[92:95]\n"
      "ds_read_b64_tr_b16 v[68:69], v23 offset:41216\n"
      "v_permlane16_swap_b32 v5, v31\n"
      "v_mfma_f32_16x16x32_bf16 a[156:159], v[74:77], v[98:101], a[156:159]\n"
      "v_max_f32 v31, v31, v5\n"
      "v_mfma_f32_16x16x32_bf16 a[28:31], v[78:81], v[86:89], a[28:31]\n"
      "ds_read_b64_tr_b16 v[70:71], v23 offset:49408\n"
      "v_mfma_f32_16x16x32_bf16 a[92:95], v[78:81], v[94:97], a[92:95]\n"
      "ds_read_b64_tr_b16 v[72:73], v23 offset:57600\n"
      "v_mfma_f32_16x16x32_bf16 a[156:159], v[78:81], v[102:105], a[156:159]\n"
      "s_waitcnt lgkmcnt(8)\n"
      "v_mfma_f32_16x16x32_bf16 a[32:35], v[50:53], v[82:85], a[32:35]\n"
      "ds_read_b64_tr_b16 v[74:75], v24 offset:33024\n"
      "v_mfma_f32_16x16x32_bf16 a[96:99], v[50:53], v[90:93], a[96:99]\n"
      "ds_read_b64_tr_b16 v[76:77], v24 offset:41216\n"
      "v_mfma_f32_16x16x32_bf16 a[160:163], v[50:53], v[98:101], a[160:163]\n"
      "v_mfma_f32_16x16x32_bf16 a[32:35], v[54:57], v[86:89], a[32:35]\n"
      "ds_read_b64_tr_b16 v[78:79], v24 offset:49408\n"
      "v_mfma_f32_16x16x32_bf16 a[96:99], v[54:57], v[94:97], a[96:99]\n"
      "ds_read_b64_tr_b16 v[80:81], v24 offset:57600\n"
      "v_mfma_f32_16x16x32_bf16 a[160:163], v[54:57], v[102:105], a[160:163]\n"
      "s_waitcnt lgkmcnt(8)\n"
      "v_mfma_f32_16x16x32_bf16 a[36:39], v[58:61], v[82:85], a[36:39]\n"
      "ds_read_b64_tr_b16 v[50:51], v25 offset:33024\n"
      "v_mfma_f32_16x16x32_bf16 a[100:103], v[58:61], v[90:93], a[100:103]\n"
      "ds_read_b64_tr_b16 v[52:53], v25 offset:41216\n"
      "v_mfma_f32_16x16x32_bf16 a[164:167], v[58:61], v[98:101], a[164:167]\n"
      "v_mfma_f32_16x16x32_bf16 a[36:39], v[62:65], v[86:89], a[36:39]\n"
      "ds_read_b64_tr_b16 v[54:55], v25 offset:49408\n"
      "v_mfma_f32_16x16x32_bf16 a[100:103], v[62:65], v[94:97], a[100:103]\n"
      "ds_read_b64_tr_b16 v[56:57], v25 offset:57600\n"
      "v_mfma_f32_16x16x32_bf16 a[164:167], v[62:65], v[102:105], a[164:167]\n"
      "s_waitcnt lgkmcnt(8)\n"
      "v_mfma_f32_16x16x32_bf16 a[40:43], v[66:69], v[82:85], a[40:43]\n"
      "ds_read_b64_tr_b16 v[58:59], v26 offset:33024\n"
      "v_mfma_f32_16x16x32_bf16 a[104:107], v[66:69], v[90:93], a[104:107]\n"
      "ds_read_b64_tr_b16 v[60:61], v26 offset:41216\n"
      "v_mfma_f32_16x16x32_bf16 a[168:171], v[66:69], v[98:101], a[168:171]\n"
      "v_mfma_f32_16x16x32_bf16 a[40:43], v[70:73], v[86:89], a[40:43]\n"
      "ds_read_b64_tr_b16 v[62:63], v26 offset:49408\n"
      "v_mfma_f32_16x16x32_bf16 a[104:107], v[70:73], v[94:97], a[104:107]\n"
      "ds_read_b64_tr_b16 v[64:65], v26 offset:57600\n"
      "v_mfma_f32_16x16x32_bf16 a[168:171], v[70:73], v[102:105], a[168:171]\n"
      "s_waitcnt lgkmcnt(8)\n"
      "v_mfma_f32_16x16x32_bf16 a[44:47], v[74:77], v[82:85], a[44:47]\n"
      "ds_read_b64_tr_b16 v[66:67], v27 offset:33024\n"
      "v_mfma_f32_16x16x32_bf16 a[108:111], v[74:77], v[90:93], a[108:111]\n"
      "ds_read_b64_tr_b16 v[68:69], v27 offset:41216\n"
      "v_mfma_f32_16x16x32_bf16 a[172:175], v[74:77], v[98:101], a[172:175]\n"
      "v_mfma_f32_16x16x32_bf16 a[44:47], v[78:81], v[86:89], a[44:47]\n"
      "ds_read_b64_tr_b16 v[70:71], v27 offset:49408\n"
      "v_mfma_f32_16x16x32_bf16 a[108:111], v[78:81], v[94:97], a[108:111]\n"
      "ds_read_b64_tr_b16 v[72:73], v27 offset:57600\n"
      "v_mfma_f32_16x16x32_bf16 a[172:175], v[78:81], v[102:105], a[172:175]\n"
      "s_waitcnt lgkmcnt(8)\n"
      "v_mfma_f32_16x16x32_bf16 a[48:51], v[50:53], v[82:85], a[48:51]\n"
      "ds_read_b64_tr_b16 v[74:75], v28 offset:33024\n"
      "v_mfma_f32_16x16x32_bf16 a[112:115], v[50:53], v[90:93], a[112:115]\n"
      "ds_read_b64_tr_b16 v[76:77], v28 offset:41216\n"
      "v_mfma_f32_16x16x32_bf16 a[176:179], v[50:53], v[98:101], a[176:179]\n"
      "v_mfma_f32_16x16x32_bf16 a[48:51], v[54:57], v[86:89], a[48:51]\n"
      "ds_read_b64_tr_b16 v[78:79], v28 offset:49408\n"
      "v_mfma_f32_16x16x32_bf16 a[112:115], v[54:57], v[94:97], a[112:115]\n"
      "ds_read_b64_tr_b16 v[80:81], v28 offset:57600\n"
      "v_mfma_f32_16x16x32_bf16 a[176:179], v[54:57], v[102:105], a[176:179]\n"
      "s_waitcnt lgkmcnt(8)\n"
      "v_mfma_f32_16x16x32_bf16 a[52:55], v[58:61], v[82:85], a[52:55]\n"
      "v_mfma_f32_16x16x32_bf16 a[116:119], v[58:61], v[90:93], a[116:119]\n"
      "v_mfma_f32_16x16x32_bf16 a[180:183], v[58:61], v[98:101], a[180:183]\n"
      "v_mfma_f32_16x16x32_bf16 a[52:55], v[62:65], v[86:89], a[52:55]\n"
      "v_mfma_f32_16x16x32_bf16 a[116:119], v[62:65], v[94:97], a[116:119]\n"
      "v_mfma_f32_16x16x32_bf16 a[180:183], v[62:65], v[102:105], a[180:183]\n"
      "s_waitcnt lgkmcnt(4)\n"
      "v_mfma_f32_16x16x32_bf16 a[56:59], v[66:69], v[82:85], a[56:59]\n"
      "v_mfma_f32_16x16x32_bf16 a[120:123], v[66:69], v[90:93], a[120:123]\n"
      "v_mfma_f32_16x16x32_bf16 a[184:187], v[66:69], v[98:101], a[184:187]\n"
      "v_mfma_f32_16x16x32_bf16 a[56:59], v[70:73], v[86:89], a[56:59]\n"
      "v_mfma_f32_16x16x32_bf16 a[120:123], v[70:73], v[94:97], a[120:123]\n"
      "v_mfma_f32_16x16x32_bf16 a[184:187], v[70:73], v[102:105], a[184:187]\n"
      "s_waitcnt lgkmcnt(2)\n"
      "v_mfma_f32_16x16x32_bf16 a[60:63], v[74:77], v[82:85], a[60:63]\n"
      "v_mfma_f32_16x16x32_bf16 a[124:127], v[74:77], v[90:93], a[124:127]\n"
      "v_mfma_f32_16x16x32_bf16 a[188:191], v[74:77], v[98:101], a[188:191]\n"
      "s_waitcnt lgkmcnt(0)\n"
      "v_mfma_f32_16x16x32_bf16 a[60:63], v[78:81], v[86:89], a[60:63]\n"
      "v_mfma_f32_16x16x32_bf16 a[124:127], v[78:81], v[94:97], a[124:127]\n"
      "v_mfma_f32_16x16x32_bf16 a[188:191], v[78:81], v[102:105], a[188:191]\n"
      "s_add_u32 s88, s88, 32768\n"
      "s_addc_u32 s89, s89, 0\n"
      "s_add_u32 s90, s90, 32768\n"
      "s_addc_u32 s91, s91, 0\n"
      "v_max3_f32 v3, v29, v30, v31\n"
      "v_cmp_lt_f32 vcc, 0x41000000, v3\n"
      "s_nop 1\n"
      "s_cbranch_vccnz SLOW2_%=\n"
      "BACK2_%=:\n"
      "s_waitcnt vmcnt(0) lgkmcnt(0)\n"
      "s_barrier\n"
      "s_sub_u32 s86, s86, 1\n"
      "s_cmp_lg_u32 s86, 0\n"
      "s_cbranch_scc1 LOOP_%=\n"
      "ds_read_b128 v[50:53], v13 offset:32768\n"
      "ds_read_b128 v[54:57], v13 offset:40960\n"
      "ds_read_b128 v[58:61], v13 offset:49152\n"
      "ds_read_b128 v[62:65], v13 offset:57344\n"
      "ds_read_b128 v[66:69], v14 offset:32768\n"
      "ds_read_b128 v[70:73], v14 offset:40960\n"
      "s_waitcnt lgkmcnt(4)\n"
      "v_mfma_f32_16x16x32_bf16 v[154:157], v[50:53], v[202:205], v[38:41]\n"
      "ds_read_b128 v[74:77], v14 offset:49152\n"
      "v_mfma_f32_16x16x32_bf16 v[170:173], v[50:53], v[234:237], v[42:45]\n"
      "v_exp_f32 v106, v106\n"
      "v_mfma_f32_16x16x32_bf16 v[186:189], v[50:53], a[224:227], v[46:49]\n"
      "v_exp_f32 v107, v107\n"
      "v_mfma_f32_16x16x32_bf16 v[158:161], v[54:57], v[202:205], v[38:41]\n"
      "ds_read_b128 v[78:81], v14 offset:57344\n"
      "v_mfma_f32_16x16x32_bf16 v[174:177], v[54:57], v[234:237], v[42:45]\n"
      "v_exp_f32 v108, v108\n"
      "v_mfma_f32_16x16x32_bf16 v[190:193], v[54:57], a[224:227], v[46:49]\n"
      "v_exp_f32 v109, v109\n"
      "s_waitcnt lgkmcnt(4)\n"
      "v_mfma_f32_16x16x32_bf16 v[162:165], v[58:61], v[202:205], v[38:41]\n"
      "ds_read_b128 v[50:53], v15 offset:32768\n"
      "v_mfma_f32_16x16x32_bf16 v[178:181], v[58:61], v[234:237], v[42:45]\n"
      "v_exp_f32 v110, v110\n"
      "v_mfma_f32_16x16x32_bf16 v[194:197], v[58:61], a[224:227], v[46:49]\n"
      "v_exp_f32 v111, v111\n"
      "v_mfma_f32_16x16x32_bf16 v[166:169], v[62:65], v[202:205], v[38:41]\n"
      "ds_read_b128 v[54:57], v15 offset:40960\n"
      "v_mfma_f32_16x16x32_bf16 v[182:185], v[62:65], v[234:237], v[42:45]\n"
      "v_exp_f32 v112, v112\n"
      "v_mfma_f32_16x16x32_bf16 v[198:201], v[62:65], a[224:227], v[46:49]\n"
      "v_exp_f32 v113, v113\n"
      "s_waitcnt lgkmcnt(4)\n"
      "v_mfma_f32_16x16x32_bf16 v[154:157], v[66:69], v[206:209], v[154:157]\n"
      "ds_read_b128 v[58:61], v15 offset:49152\n"
      "v_mfma_f32_16x16x32_bf16 v[170:173], v[66:69], v[238:241], v[170:173]\n"
      "v_cvt_pk_bf16_f32 v82, v106, v107\n"
      "v_cvt_pk_bf16_f32 v83, v108, v109\n"
      "v_mfma_f32_16x16x32_bf16 v[186:189], v[66:69], a[228:231], v[186:189]\n"
      "v_cvt_pk_bf16_f32 v84, v110, v111\n"
      "v_cvt_pk_bf16_f32 v85, v112, v113\n"
      "v_mfma_f32_16x16x32_bf16 v[158:161], v[70:73], v[206:209], v[158:161]\n"
      "ds_read_b128 v[62:65], v15 offset:57344\n"
      "v_mfma_f32_16x16x32_bf16 v[174:177], v[70:73], v[238:241], v[174:177]\n"
      "v_exp_f32 v114, v114\n"
      "v_mfma_f32_16x16x32_bf16 v[190:193], v[70:73], a[228:231], v[190:193]\n"
      "v_exp_f32 v115, v115\n"
      "s_waitcnt lgkmcnt(4)\n"
      "v_mfma_f32_16x16x32_bf16 v[162:165], v[74:77], v[206:209], v[162:165]\n"
      "ds_read_b128 v[66:69], v16 offset:32768\n"
      "v_mfma_f32_16x16x32_bf16 v[178:181], v[74:77], v[238:241], v[178:181]\n"
      "v_exp_f32 v116, v116\n"
      "v_mfma_f32_16x16x32_bf16 v[194:197], v[74:77], a[228:231], v[194:197]\n"
      "v_exp_f32 v117, v117\n"
      "v_mfma_f32_16x16x32_bf16 v[166:169], v[78:81], v[206:209], v[166:169]\n"
      "ds_read_b128 v[70:73], v16 offset:40960\n"
      "v_mfma_f32_16x16x32_bf16 v[182:185], v[78:81], v[238:241], v[182:185]\n"
      "v_exp_f32 v118, v118\n"
      "v_mfma_f32_16x16x32_bf16 v[198:201], v[78:81], a[228:231], v[198:201]\n"
      "v_exp_f32 v119, v119\n"
      "s_waitcnt lgkmcnt(4)\n"
      "v_mfma_f32_16x16x32_bf16 v[154:157], v[50:53], v[210:213], v[154:157]\n"
      "ds_read_b128 v[74:77], v16 offset:49152\n"
      "v_mfma_f32_16x16x32_bf16 v[170:173], v[50:53], v[242:245], v[170:173]\n"
      "v_exp_f32 v120, v120\n"
      "v_mfma_f32_16x16x32_bf16 v[186:189], v[50:53], a[232:235], v[186:189]\n"
      "v_exp_f32 v121, v121\n"
      "v_mfma_f32_16x16x32_bf16 v[158:161], v[54:57], v[210:213], v[158:161]\n"
      "ds_read_b128 v[78:81], v16 offset:57344\n"
      "v_mfma_f32_16x16x32_bf16 v[174:177], v[54:57], v[242:245], v[174:177]\n"
      "v_cvt_pk_bf16_f32 v86, v114, v115\n"
      "v_cvt_pk_bf16_f32 v87, v116, v117\n"
      "v_mfma_f32_16x16x32_bf16 v[190:193], v[54:57], a[232:235], v[190:193]\n"
      "v_cvt_pk_bf16_f32 v88, v118, v119\n"
      "v_cvt_pk_bf16_f32 v89, v120, v121\n"
      "s_waitcnt lgkmcnt(4)\n"
      "v_mfma_f32_16x16x32_bf16 v[162:165], v[58:61], v[210:213], v[162:165]\n"
      "ds_read_b128 v[50:53], v17 offset:32768\n"
      "v_mfma_f32_16x16x32_bf16 v[178:181], v[58:61], v[242:245], v[178:181]\n"
      "v_exp_f32 v122, v122\n"
      "v_mfma_f32_16x16x32_bf16 v[194:197], v[58:61], a[232:235], v[194:197]\n"
      "v_exp_f32 v123, v123\n"
      "v_mfma_f32_16x16x32_bf16 v[166:169], v[62:65], v[210:213], v[166:169]\n"
      "ds_read_b128 v[54:57], v17 offset:40960\n"
      "v_mfma_f32_16x16x32_bf16 v[182:185], v[62:65], v[242:245], v[182:185]\n"
      "v_exp_f32 v124, v124\n"
      "v_mfma_f32_16x16x32_bf16 v[198:201], v[62:65], a[232:235], v[198:201]\n"
      "v_exp_f32 v125, v125\n"
      "s_waitcnt lgkmcnt(4)\n"
      "v_mfma_f32_16x16x32_bf16 v[154:157], v[66:69], v[214:217], v[154:157]\n"
      "ds_read_b128 v[58:61], v17 offset:49152\n"
      "v_mfma_f32_16x16x32_bf16 v[170:173], v[66:69], a[204:207], v[170:173]\n"
      "v_exp_f32 v126, v126\n"
      "v_mfma_f32_16x16x32_bf16 v[186:189], v[66:69], a[236:239], v[186:189]\n"
      "v_exp_f32 v127, v127\n"
      "v_mfma_f32_16x16x32_bf16 v[158:161], v[70:73], v[214:217], v[158:161]\n"
      "ds_read_b128 v[62:65], v17 offset:57344\n"
      "v_mfma_f32_16x16x32_bf16 v[174:177], v[70:73], a[204:207], v[174:177]\n"
      "v_exp_f32 v128, v128\n"
      "v_mfma_f32_16x16x32_bf16 v[190:193], v[70:73], a[236:239], v[190:193]\n"
      "v_exp_f32 v129, v129\n"
      "s_waitcnt lgkmcnt(4)\n"
      "v_mfma_f32_16x16x32_bf16 v[162:165], v[74:77], v[214:217], v[162:165]\n"
      "ds_read_b128 v[66:69], v18 offset:32768\n"
      "v_mfma_f32_16x16x32_bf16 v[178:181], v[74:77], a[204:207], v[178:181]\n"
      "v_cvt_pk_bf16_f32 v90, v122, v123\n"
      "v_cvt_pk_bf16_f32 v91, v124, v125\n"
      "v_mfma_f32_16x16x32_bf16 v[194:197], v[74:77], a[236:239], v[194:197]\n"
      "v_cvt_pk_bf16_f32 v92, v126, v127\n"
      "v_cvt_pk_bf16_f32 v93, v128, v129\n"
      "v_mfma_f32_16x16x32_bf16 v[166:169], v[78:81], v[214:217], v[166:169]\n"
      "ds_read_b128 v[70:73], v18 offset:40960\n"
      "v_mfma_f32_16x16x32_bf16 v[182:185], v[78:81], a[204:207], v[182:185]\n"
      "v_exp_f32 v130, v130\n"
      "v_mfma_f32_16x16x32_bf16 v[198:201], v[78:81], a[236:239], v[198:201]\n"
      "v_exp_f32 v131, v131\n"
      "s_waitcnt lgkmcnt(4)\n"
      "v_mfma_f32_16x16x32_bf16 v[154:157], v[50:53], v[218:221], v[154:157]\n"
      "ds_read_b128 v[74:77], v18 offset:49152\n"
      "v_mfma_f32_16x16x32_bf16 v[170:173], v[50:53], a[208:211], v[170:173]\n"
      "v_exp_f32 v132, v132\n"
      "v_mfma_f32_16x16x32_bf16 v[186:189], v[50:53], a[240:243], v[186:189]\n"
      "v_exp_f32 v133, v133\n"
      "v_mfma_f32_16x16x32_bf16 v[158:161], v[54:57], v[218:221], v[158:161]\n"
      "ds_read_b128 v[78:81], v18 offset:57344\n"
      "v_mfma_f32_16x16x32_bf16 v[174:177], v[54:57], a[208:211], v[174:177]\n"
      "v_exp_f32 v134, v134\n"
      "v_mfma_f32_16x16x32_bf16 v[190:193], v[54:57], a[240:243], v[190:193]\n"
      "v_exp_f32 v135, v135\n"
      "s_waitcnt lgkmcnt(4)\n"
      "v_mfma_f32_16x16x32_bf16 v[162:165], v[58:61], v[218:221], v[162:165]\n"
      "ds_read_b128 v[50:53], v19 offset:32768\n"
      "v_mfma_f32_16x16x32_bf16 v[178:181], v[58:61], a[208:211], v[178:181]\n"
      "v_exp_f32 v136, v136\n"
      "v_mfma_f32_16x16x32_bf16 v[194:197], v[58:61], a[240:243], v[194:197]\n"
      "v_exp_f32 v137, v137\n"
      "v_mfma_f32_16x16x32_bf16 v[166:169], v[62:65], v[218:221], v[166:169]\n"
      "ds_read_b128 v[54:57], v19 offset:40960\n"
      "v_mfma_f32_16x16x32_bf16 v[182:185], v[62:65], a[208:211], v[182:185]\n"
      "v_cvt_pk_bf16_f32 v94, v130, v131\n"
      "v_cvt_pk_bf16_f32 v95, v132, v133\n"
      "v_mfma_f32_16x16x32_bf16 v[198:201], v[62:65], a[240:243], v[198:201]\n"
      "v_cvt_pk_bf16_f32 v96, v134, v135\n"
      "v_cvt_pk_bf16_f32 v97, v136, v137\n"
      "s_waitcnt lgkmcnt(4)\n"
      "v_mfma_f32_16x16x32_bf16 v[154:157], v[66:69], v[222:225], v[154:157]\n"
      "ds_read_b128 v[58:61], v19 offset:49152\n"
      "v_mfma_f32_16x16x32_bf16 v[170:173], v[66:69], a[212:215], v[170:173]\n"
      "v_exp_f32 v138, v138\n"
      "v_mfma_f32_16x16x32_bf16 v[186:189], v[66:69], a[244:247], v[186:189]\n"
      "v_exp_f32 v139, v139\n"
      "v_mfma_f32_16x16x32_bf16 v[158:161], v[70:73], v[222:225], v[158:161]\n"
      "ds_read_b128 v[62:65], v19 offset:57344\n"
      "v_mfma_f32_16x16x32_bf16 v[174:177], v[70:73], a[212:215], v[174:177]\n"
      "v_exp_f32 v140, v140\n"
      "v_mfma_f32_16x16x32_bf16 v[190:193], v[70:73], a[244:247], v[190:193]\n"
      "v_exp_f32 v141, v141\n"
      "s_waitcnt lgkmcnt(4)\n"
      "v_mfma_f32_16x16x32_bf16 v[162:165], v[74:77], v[222:225], v[162:165]\n"
      "ds_read_b128 v[66:69], v20 offset:32768\n"
      "v_mfma_f32_16x16x32_bf16 v[178:181], v[74:77], a[212:215], v[178:181]\n"
      "v_exp_f32 v142, v142\n"
      "v_mfma_f32_16x16x32_bf16 v[194:197], v[74:77], a[244:247], v[194:197]\n"
      "v_exp_f32 v143, v143\n"
      "v_mfma_f32_16x16x32_bf16 v[166:169], v[78:81], v[222:225], v[166:169]\n"
      "ds_read_b128 v[70:73], v20 offset:40960\n"
      "v_mfma_f32_16x16x32_bf16 v[182:185], v[78:81], a[212:215], v[182:185]\n"
      "v_exp_f32 v144, v144\n"
      "v_mfma_f32_16x16x32_bf16 v[198:201], v[78:81], a[244:247], v[198:201]\n"
      "v_exp_f32 v145, v145\n"
      "s_waitcnt lgkmcnt(4)\n"
      "v_mfma_f32_16x16x32_bf16 v[154:157], v[50:53], v[226:229], v[154:157]\n"
      "ds_read_b128 v[74:77], v20 offset:49152\n"
      "v_mfma_f32_16x16x32_bf16 v[170:173], v[50:53], a[216:219], v[170:173]\n"
      "v_cvt_pk_bf16_f32 v98, v138, v139\n"
      "v_cvt_pk_bf16_f32 v99, v140, v141\n"
      "v_mfma_f32_16x16x32_bf16 v[186:189], v[50:53], a[248:251], v[186:189]\n"
      "v_cvt_pk_bf16_f32 v100, v142, v143\n"
      "v_cvt_pk_bf16_f32 v101, v144, v145\n"
      "v_mfma_f32_16x16x32_bf16 v[158:161], v[54:57], v[226:229], v[158:161]\n"
      "ds_read_b128 v[78:81], v20 offset:57344\n"
      "v_mfma_f32_16x16x32_bf16 v[174:177], v[54:57], a[216:219], v[174:177]\n"
      "v_exp_f32 v146, v146\n"
      "v_mfma_f32_16x16x32_bf16 v[190:193], v[54:57], a[248:251], v[190:193]\n"
      "v_exp_f32 v147, v147\n"
      "s_waitcnt lgkmcnt(4)\n"
      "v_mfma_f32_16x16x32_bf16 v[162:165], v[58:61], v[226:229], v[162:165]\n"
      "ds_read_b64_tr_b16 v[50:51], v21 offset:0\n"
      "ds_read_b64_tr_b16 v[52:53], v21 offset:8192\n"
      "v_mfma_f32_16x16x32_bf16 v[178:181], v[58:61], a[216:219], v[178:181]\n"
      "v_exp_f32 v148, v148\n"
      "v_mfma_f32_16x16x32_bf16 v[194:197], v[58:61], a[248:251], v[194:197]\n"
      "v_exp_f32 v149, v149\n"
      "v_mfma_f32_16x16x32_bf16 v[166:169], v[62:65], v[226:229], v[166:169]\n"
      "ds_read_b64_tr_b16 v[54:55], v21 offset:16384\n"
      "ds_read_b64_tr_b16 v[56:57], v21 offset:24576\n"
      "v_mfma_f32_16x16x32_bf16 v[182:185], v[62:65], a[216:219], v[182:185]\n"
      "v_exp_f32 v150, v150\n"
      "v_mfma_f32_16x16x32_bf16 v[198:201], v[62:65], a[248:251], v[198:201]\n"
      "v_exp_f32 v151, v151\n"
      "s_waitcnt lgkmcnt(6)\n"
      "v_mfma_f32_16x16x32_bf16 v[154:157], v[66:69], v[230:233], v[154:157]\n"
      "ds_read_b64_tr_b16 v[58:59], v22 offset:0\n"
      "ds_read_b64_tr_b16 v[60:61], v22 offset:8192\n"
      "v_mfma_f32_16x16x32_bf16 v[170:173], v[66:69], a[220:223], v[170:173]\n"
      "v_exp_f32 v152, v152\n"
      "v_mfma_f32_16x16x32_bf16 v[186:189], v[66:69], a[252:255], v[186:189]\n"
      "v_exp_f32 v153, v153\n"
      "v_mfma_f32_16x16x32_bf16 v[158:161], v[70:73], v[230:233], v[158:161]\n"
      "ds_read_b64_tr_b16 v[62:63], v22 offset:16384\n"
      "ds_read_b64_tr_b16 v[64:65], v22 offset:24576\n"
      "v_mfma_f32_16x16x32_bf16 v[174:177], v[70:73], a[220:223], v[174:177]\n"
      "v_cvt_pk_bf16_f32 v102, v146, v147\n"
      "v_cvt_pk_bf16_f32 v103, v148, v149\n"
      "v_mfma_f32_16x16x32_bf16 v[190:193], v[70:73], a[252:255], v[190:193]\n"
      "v_cvt_pk_bf16_f32 v104, v150, v151\n"
      "v_cvt_pk_bf16_f32 v105, v152, v153\n"
      "s_waitcnt lgkmcnt(8)\n"
      "v_mfma_f32_16x16x32_bf16 v[162:165], v[74:77], v[230:233], v[162:165]\n"
      "ds_read_b64_tr_b16 v[66:67], v23 offset:0\n"
      "ds_read_b64_tr_b16 v[68:69], v23 offset:8192\n"
      "v_mfma_f32_16x16x32_bf16 v[178:181], v[74:77], a[220:223], v[178:181]\n"
      "v_mfma_f32_16x16x32_bf16 v[194:197], v[74:77], a[252:255], v[194:197]\n"
      "v_mfma_f32_16x16x32_bf16 v[166:169], v[78:81], v[230:233], v[166:169]\n"
      "ds_read_b64_tr_b16 v[70:71], v23 offset:16384\n"
      "ds_read_b64_tr_b16 v[72:73], v23 offset:24576\n"
      "v_mfma_f32_16x16x32_bf16 v[182:185], v[78:81], a[220:223], v[182:185]\n"
      "v_mfma_f32_16x16x32_bf16 v[198:201], v[78:81], a[252:255], v[198:201]\n"
      "v_mfma_f32_16x16x32_bf16 a[192:195], v[34:37], v[82:85], a[192:195]\n"
      "s_add_u32 m0, %4, 98304\n"
      "v_xor_b32 v10, 0, v12\n"
      "global_load_lds_dwordx4 v10, s[90:91]\n"
      "v_mfma_f32_16x16x32_bf16 a[192:195], v[34:37], v[86:89], a[192:195]\n"
      "s_add_u32 m0, m0, 1024\n"
      "v_xor_b32 v10, 1088, v12\n"
      "global_load_lds_dwordx4 v10, s[90:91]\n"
      "v_mfma_f32_16x16x32_bf16 a[196:199], v[34:37], v[90:93], a[196:199]\n"
      "s_add_u32 m0, m0, 1024\n"
      "v_xor_b32 v10, 2176, v12\n"
      "global_load_lds_dwordx4 v10, s[90:91]\n"
      "v_mfma_f32_16x16x32_bf16 a[196:199], v[34:37], v[94:97], a[196:199]\n"
      "s_add_u32 m0, m0, 1024\n"
      "v_xor_b32 v10, 3264, v12\n"
      "global_load_lds_dwordx4 v10, s[90:91]\n"
      "v_mfma_f32_16x16x32_bf16 a[200:203], v[34:37], v[98:101], a[200:203]\n"
      "s_add_u32 m0, m0, 1024\n"
      "v_xor_b32 v10, 4096, v12\n"
      "global_load_lds_dwordx4 v10, s[90:91]\n"
      "v_mfma_f32_16x16x32_bf16 a[200:203], v[34:37], v[102:105], a[200:203]\n"
      "s_add_u32 m0, m0, 1024\n"
      "v_xor_b32 v10, 5184, v12\n"
      "global_load_lds_dwordx4 v10, s[90:91]\n"
      "s_waitcnt lgkmcnt(8)\n"
      "v_mfma_f32_16x16x32_bf16 a[0:3], v[50:53], v[82:85], a[0:3]\n"
      "ds_read_b64_tr_b16 v[74:75], v24 offset:0\n"
      "v_mfma_f32_16x16x32_bf16 a[64:67], v[50:53], v[90:93], a[64:67]\n"
      "ds_read_b64_tr_b16 v[76:77], v24 offset:8192\n"
      "s_add_u32 m0, m0, 1024\n"
      "v_xor_b32 v10, 6272, v12\n"
      "global_load_lds_dwordx4 v10, s[90:91]\n"
      "v_mfma_f32_16x16x32_bf16 a[128:131], v[50:53], v[98:101], a[128:131]\n"
      "s_add_u32 m0, m0, 1024\n"
      "v_xor_b32 v10, 7360, v12\n"
      "global_load_lds_dwordx4 v10, s[90:91]\n"
      "v_max3_f32 v29, v154, v155, v156\n"
      "v_mfma_f32_16x16x32_bf16 a[0:3], v[54:57], v[86:89], a[0:3]\n"
      "ds_read_b64_tr_b16 v[78:79], v24 offset:16384\n"
      "v_mfma_f32_16x16x32_bf16 a[64:67], v[54:57], v[94:97], a[64:67]\n"
      "ds_read_b64_tr_b16 v[80:81], v24 offset:24576\n"
      "v_max3_f32 v29, v29, v157, v158\n"
      "v_mfma_f32_16x16x32_bf16 a[128:131], v[54:57], v[102:105], a[128:131]\n"
      "v_max3_f32 v29, v29, v159, v160\n"
      "v_max3_f32 v29, v29, v161, v162\n"
      "s_waitcnt lgkmcnt(8)\n"
      "v_mfma_f32_16x16x32_bf16 a[4:7], v[58:61], v[82:85], a[4:7]\n"
      "ds_read_b64_tr_b16 v[50:51], v25 offset:0\n"
      "v_mfma_f32_16x16x32_bf16 a[68:71], v[58:61], v[90:93], a[68:71]\n"
      "ds_read_b64_tr_b16 v[52:53], v25 offset:8192\n"
      "v_max3_f32 v29, v29, v163, v164\n"
      "v_mfma_f32_16x16x32_bf16 a[132:135], v[58:61], v[98:101], a[132:135]\n"
      "v_max3_f32 v29, v29, v165, v166\n"
      "v_max3_f32 v29, v29, v167, v168\n"
      "v_mfma_f32_16x16x32_bf16 a[4:7], v[62:65], v[86:89], a[4:7]\n"
      "ds_read_b64_tr_b16 v[54:55], v25 offset:16384\n"
      "v_mfma_f32_16x16x32_bf16 a[68:71], v[62:65], v[94:97], a[68:71]\n"
      "ds_read_b64_tr_b16 v[56:57], v25 offset:24576\n"
      "v_max_f32 v29, v29, v169\n"
      "v_mfma_f32_16x16x32_bf16 a[132:135], v[62:65], v[102:105], a[132:135]\n"
      "v_mov_b32 v3, v29\n"
      "s_nop 1\n"
      "v_permlane32_swap_b32 v3, v29\n"
      "s_waitcnt lgkmcnt(8)\n"
      "v_mfma_f32_16x16x32_bf16 a[8:11], v[66:69], v[82:85], a[8:11]\n"
      "ds_read_b64_tr_b16 v[58:59], v26 offset:0\n"
      "v_mfma_f32_16x16x32_bf16 a[72:75], v[66:69], v[90:93], a[72:75]\n"
      "ds_read_b64_tr_b16 v[60:61], v26 offset:8192\n"
      "v_max_f32 v29, v29, v3\n"
      "v_mfma_f32_16x16x32_bf16 a[136:139], v[66:69], v[98:101], a[136:139]\n"
      "v_mov_b32 v3, v29\n"
      "s_nop 1\n"
      "v_permlane16_swap_b32 v3, v29\n"
      "v_mfma_f32_16x16x32_bf16 a[8:11], v[70:73], v[86:89], a[8:11]\n"
      "ds_read_b64_tr_b16 v[62:63], v26 offset:16384\n"
      "v_mfma_f32_16x16x32_bf16 a[72:75], v[70:73], v[94:97], a[72:75]\n"
      "ds_read_b64_tr_b16 v[64:65], v26 offset:24576\n"
      "v_max_f32 v29, v29, v3\n"
      "v_mfma_f32_16x16x32_bf16 a[136:139], v[70:73], v[102:105], a[136:139]\n"
      "v_max3_f32 v30, v170, v171, v172\n"
      "v_max3_f32 v30, v30, v173, v174\n"
      "s_waitcnt lgkmcnt(8)\n"
      "v_mfma_f32_16x16x32_bf16 a[12:15], v[74:77], v[82:85], a[12:15]\n"
      "ds_read_b64_tr_b16 v[66:67], v27 offset:0\n"
      "v_mfma_f32_16x16x32_bf16 a[76:79], v[74:77], v[90:93], a[76:79]\n"
      "ds_read_b64_tr_b16 v[68:69], v27 offset:8192\n"
      "v_max3_f32 v30, v30, v175, v176\n"
      "v_mfma_f32_16x16x32_bf16 a[140:143], v[74:77], v[98:101], a[140:143]\n"
      "v_max3_f32 v30, v30, v177, v178\n"
      "v_max3_f32 v30, v30, v179, v180\n"
      "v_mfma_f32_16x16x32_bf16 a[12:15], v[78:81], v[86:89], a[12:15]\n"
      "ds_read_b64_tr_b16 v[70:71], v27 offset:16384\n"
      "v_mfma_f32_16x16x32_bf16 a[76:79], v[78:81], v[94:97], a[76:79]\n"
      "ds_read_b64_tr_b16 v[72:73], v27 offset:24576\n"
      "v_max3_f32 v30, v30, v181, v182\n"
      "v_mfma_f32_16x16x32_bf16 a[140:143], v[78:81], v[102:105], a[140:143]\n"
      "v_max3_f32 v30, v30, v183, v184\n"
      "v_max_f32 v30, v30, v185\n"
      "s_waitcnt lgkmcnt(8)\n"
      "v_mfma_f32_16x16x32_bf16 a[16:19], v[50:53], v[82:85], a[16:19]\n"
      "ds_read_b64_tr_b16 v[74:75], v28 offset:0\n"
      "v_mfma_f32_16x16x32_bf16 a[80:83], v[50:53], v[90:93], a[80:83]\n"
      "ds_read_b64_tr_b16 v[76:77], v28 offset:8192\n"
      "v_mov_b32 v4, v30\n"
      "v_mfma_f32_16x16x32_bf16 a[144:147], v[50:53], v[98:101], a[144:147]\n"
      "s_nop 0\n"
      "v_permlane32_swap_b32 v4, v30\n"
      "v_max_f32 v30, v30, v4\n"
      "v_mfma_f32_16x16x32_bf16 a[16:19], v[54:57], v[86:89], a[16:19]\n"
      "ds_read_b64_tr_b16 v[78:79], v28 offset:16384\n"
      "v_mfma_f32_16x16x32_bf16 a[80:83], v[54:57], v[94:97], a[80:83]\n"
      "ds_read_b64_tr_b16 v[80:81], v28 offset:24576\n"
      "v_mov_b32 v4, v30\n"
      "v_mfma_f32_16x16x32_bf16 a[144:147], v[54:57], v[102:105], a[144:147]\n"
      "s_nop 0\n"
      "v_permlane16_swap_b32 v4, v30\n"
      "v_max_f32 v30, v30, v4\n"
      "s_waitcnt lgkmcnt(8)\n"
      "v_mfma_f32_16x16x32_bf16 a[20:23], v[58:61], v[82:85], a[20:23]\n"
      "ds_read_b64_tr_b16 v[50:51], v21 offset:256\n"
      "v_mfma_f32_16x16x32_bf16 a[84:87], v[58:61], v[90:93], a[84:87]\n"
      "ds_read_b64_tr_b16 v[52:53], v21 offset:8448\n"
      "v_max3_f32 v31, v186, v187, v188\n"
      "v_mfma_f32_16x16x32_bf16 a[148:151], v[58:61], v[98:101], a[148:151]\n"
      "v_max3_f32 v31, v31, v189, v190\n"
      "v_max3_f32 v31, v31, v191, v192\n"
      "v_mfma_f32_16x16x32_bf16 a[20:23], v[62:65], v[86:89], a[20:23]\n"
      "ds_read_b64_tr_b16 v[54:55], v21 offset:16640\n"
      "v_mfma_f32_16x16x32_bf16 a[84:87], v[62:65], v[94:97], a[84:87]\n"
      "ds_read_b64_tr_b16 v[56:57], v21 offset:24832\n"
      "v_max3_f32 v31, v31, v193, v194\n"
      "v_mfma_f32_16x16x32_bf16 a[148:151], v[62:65], v[102:105], a[148:151]\n"
      "v_max3_f32 v31, v31, v195, v196\n"
      "v_max3_f32 v31, v31, v197, v198\n"
      "s_waitcnt lgkmcnt(8)\n"
      "v_mfma_f32_16x16x32_bf16 a[24:27], v[66:69], v[82:85], a[24:27]\n"
      "ds_read_b64_tr_b16 v[58:59], v22 offset:256\n"
      "v_mfma_f32_16x16x32_bf16 a[88:91], v[66:69], v[90:93], a[88:91]\n"
      "ds_read_b64_tr_b16 v[60:61], v22 offset:8448\n"
      "v_max3_f32 v31, v31, v199, v200\n"
      "v_mfma_f32_16x16x32_bf16 a[152:155], v[66:69], v[98:101], a[152:155]\n"
      "v_max_f32 v31, v31, v201\n"
      "v_mov_b32 v5, v31\n"
      "v_mfma_f32_16x16x32_bf16 a[24:27], v[70:73], v[86:89], a[24:27]\n"
      "ds_read_b64_tr_b16 v[62:63], v22 offset:16640\n"
      "v_mfma_f32_16x16x32_bf16 a[88:91], v[70:73], v[94:97], a[88:91]\n"
      "ds_read_b64_tr_b16 v[64:65], v22 offset:24832\n"
      "v_permlane32_swap_b32 v5, v31\n"
      "v_mfma_f32_16x16x32_bf16 a[152:155], v[70:73], v[102:105], a[152:155]\n"
      "v_max_f32 v31, v31, v5\n"
      "v_mov_b32 v5, v31\n"
      "s_waitcnt lgkmcnt(8)\n"
      "v_mfma_f32_16x16x32_bf16 a[28:31], v[74:77], v[82:85], a[28:31]\n"
      "ds_read_b64_tr_b16 v[66:67], v23 offset:256\n"
      "v_mfma_f32_16x16x32_bf16 a[92:95], v[74:77], v[90:93], a[92:95]\n"
      "ds_read_b64_tr_b16 v[68:69], v23 offset:8448\n"
      "v_permlane16_swap_b32 v5, v31\n"
      "v_mfma_f32_16x16x32_bf16 a[156:159], v[74:77], v[98:101], a[156:159]\n"
      "v_max_f32 v31, v31, v5\n"
      "v_mfma_f32_16x16x32_bf16 a[28:31], v[78:81], v[86:89], a[28:31]\n"
      "ds_read_b64_tr_b16 v[70:71], v23 offset:16640\n"
      "v_mfma_f32_16x16x32_bf16 a[92:95], v[78:81], v[94:97], a[92:95]\n"
      "ds_read_b64_tr_b16 v[72:73], v23 offset:24832\n"
      "v_mfma_f32_16x16x32_bf16 a[156:159], v[78:81], v[102:105], a[156:159]\n"
      "s_waitcnt lgkmcnt(8)\n"
      "v_mfma_f32_16x16x32_bf16 a[32:35], v[50:53], v[82:85], a[32:35]\n"
      "ds_read_b64_tr_b16 v[74:75], v24 offset:256\n"
      "v_mfma_f32_16x16x32_bf16 a[96:99], v[50:53], v[90:93], a[96:99]\n"
      "ds_read_b64_tr_b16 v[76:77], v24 offset:8448\n"
      "v_mfma_f32_16x16x32_bf16 a[160:163], v[50:53], v[98:101], a[160:163]\n"
      "v_mfma_f32_16x16x32_bf16 a[32:35], v[54:57], v[86:89], a[32:35]\n"
      "ds_read_b64_tr_b16 v[78:79], v24 offset:16640\n"
      "v_mfma_f32_16x16x32_bf16 a[96:99], v[54:57], v[94:97], a[96:99]\n"
      "ds_read_b64_tr_b16 v[80:81], v24 offset:24832\n"
      "v_mfma_f32_16x16x32_bf16 a[160:163], v[54:57], v[102:105], a[160:163]\n"
      "s_waitcnt lgkmcnt(8)\n"
      "v_mfma_f32_16x16x32_bf16 a[36:39], v[58:61], v[82:85], a[36:39]\n"
      "ds_read_b64_tr_b16 v[50:51], v25 offset:256\n"
      "v_mfma_f32_16x16x32_bf16 a[100:103], v[58:61], v[90:93], a[100:103]\n"
      "ds_read_b64_tr_b16 v[52:53], v25 offset:8448\n"
      "v_mfma_f32_16x16x32_bf16 a[164:167], v[58:61], v[98:101], a[164:167]\n"
      "v_mfma_f32_16x16x32_bf16 a[36:39], v[62:65], v[86:89], a[36:39]\n"
      "ds_read_b64_tr_b16 v[54:55], v25 offset:16640\n"
      "v_mfma_f32_16x16x32_bf16 a[100:103], v[62:65], v[94:97], a[100:103]\n"
      "ds_read_b64_tr_b16 v[56:57], v25 offset:24832\n"
      "v_mfma_f32_16x16x32_bf16 a[164:167], v[62:65], v[102:105], a[164:167]\n"
      "s_waitcnt lgkmcnt(8)\n"
      "v_mfma_f32_16x16x32_bf16 a[40:43], v[66:69], v[82:85], a[40:43]\n"
      "ds_read_b64_tr_b16 v[58:59], v26 offset:256\n"
      "v_mfma_f32_16x16x32_bf16 a[104:107], v[66:69], v[90:93], a[104:107]\n"
      "ds_read_b64_tr_b16 v[60:61], v26 offset:8448\n"
      "v_mfma_f32_16x16x32_bf16 a[168:171], v[66:69], v[98:101], a[168:171]\n"
      "v_mfma_f32_16x16x32_bf16 a[40:43], v[70:73], v[86:89], a[40:43]\n"
      "ds_read_b64_tr_b16 v[62:63], v26 offset:16640\n"
      "v_mfma_f32_16x16x32_bf16 a[104:107], v[70:73], v[94:97], a[104:107]\n"
      "ds_read_b64_tr_b16 v[64:65], v26 offset:24832\n"
      "v_mfma_f32_16x16x32_bf16 a[168:171], v[70:73], v[102:105], a[168:171]\n"
      "s_waitcnt lgkmcnt(8)\n"
      "v_mfma_f32_16x16x32_bf16 a[44:47], v[74:77], v[82:85], a[44:47]\n"
      "ds_read_b64_tr_b16 v[66:67], v27 offset:256\n"
      "v_mfma_f32_16x16x32_bf16 a[108:111], v[74:77], v[90:93], a[108:111]\n"
      "ds_read_b64_tr_b16 v[68:69], v27 offset:8448\n"
      "v_mfma_f32_16x16x32_bf16 a[172:175], v[74:77], v[98:101], a[172:175]\n"
      "v_mfma_f32_16x16x32_bf16 a[44:47], v[78:81], v[86:89], a[44:47]\n"
      "ds_read_b64_tr_b16 v[70:71], v27 offset:16640\n"
      "v_mfma_f32_16x16x32_bf16 a[108:111], v[78:81], v[94:97], a[108:111]\n"
      "ds_read_b64_tr_b16 v[72:73], v27 offset:24832\n"
      "v_mfma_f32_16x16x32_bf16 a[172:175], v[78:81], v[102:105], a[172:175]\n"
      "s_waitcnt lgkmcnt(8)\n"
      "v_mfma_f32_16x16x32_bf16 a[48:51], v[50:53], v[82:85], a[48:51]\n"
      "ds_read_b64_tr_b16 v[74:75], v28 offset:256\n"
      "v_mfma_f32_16x16x32_bf16 a[112:115], v[50:53], v[90:93], a[112:115]\n"
      "ds_read_b64_tr_b16 v[76:77], v28 offset:8448\n"
      "v_mfma_f32_16x16x32_bf16 a[176:179], v[50:53], v[98:101], a[176:179]\n"
      "v_mfma_f32_16x16x32_bf16 a[48:51], v[54:57], v[86:89], a[48:51]\n"
      "ds_read_b64_tr_b16 v[78:79], v28 offset:16640\n"
      "v_mfma_f32_16x16x32_bf16 a[112:115], v[54:57], v[94:97], a[112:115]\n"
      "ds_read_b64_tr_b16 v[80:81], v28 offset:24832\n"
      "v_mfma_f32_16x16x32_bf16 a[176:179], v[54:57], v[102:105], a[176:179]\n"
      "s_waitcnt lgkmcnt(8)\n"
      "v_mfma_f32_16x16x32_bf16 a[52:55], v[58:61], v[82:85], a[52:55]\n"
      "v_mfma_f32_16x16x32_bf16 a[116:119], v[58:61], v[90:93], a[116:119]\n"
      "v_mfma_f32_16x16x32_bf16 a[180:183], v[58:61], v[98:101], a[180:183]\n"
      "v_mfma_f32_16x16x32_bf16 a[52:55], v[62:65], v[86:89], a[52:55]\n"
      "v_mfma_f32_16x16x32_bf16 a[116:119], v[62:65], v[94:97], a[116:119]\n"
      "v_mfma_f32_16x16x32_bf16 a[180:183], v[62:65], v[102:105], a[180:183]\n"
      "s_waitcnt lgkmcnt(4)\n"
      "v_mfma_f32_16x16x32_bf16 a[56:59], v[66:69], v[82:85], a[56:59]\n"
      "v_mfma_f32_16x16x32_bf16 a[120:123], v[66:69], v[90:93], a[120:123]\n"
      "v_mfma_f32_16x16x32_bf16 a[184:187], v[66:69], v[98:101], a[184:187]\n"
      "v_mfma_f32_16x16x32_bf16 a[56:59], v[70:73], v[86:89], a[56:59]\n"
      "v_mfma_f32_16x16x32_bf16 a[120:123], v[70:73], v[94:97], a[120:123]\n"
      "v_mfma_f32_16x16x32_bf16 a[184:187], v[70:73], v[102:105], a[184:187]\n"
      "s_waitcnt lgkmcnt(2)\n"
      "v_mfma_f32_16x16x32_bf16 a[60:63], v[74:77], v[82:85], a[60:63]\n"
      "v_mfma_f32_16x16x32_bf16 a[124:127], v[74:77], v[90:93], a[124:127]\n"
      "v_mfma_f32_16x16x32_bf16 a[188:191], v[74:77], v[98:101], a[188:191]\n"
      "s_waitcnt lgkmcnt(0)\n"
      "v_mfma_f32_16x16x32_bf16 a[60:63], v[78:81], v[86:89], a[60:63]\n"
      "v_mfma_f32_16x16x32_bf16 a[124:127], v[78:81], v[94:97], a[124:127]\n"
      "v_mfma_f32_16x16x32_bf16 a[188:191], v[78:81], v[102:105], a[188:191]\n"
      "s_add_u32 s90, s90, 32768\n"
      "s_addc_u32 s91, s91, 0\n"
      "v_max3_f32 v3, v29, v30, v31\n"
      "v_cmp_lt_f32 vcc, 0x41000000, v3\n"
      "s_nop 1\n"
      "s_cbranch_vccnz SLOW3_%=\n"
      "BACK3_%=:\n"
      "s_waitcnt vmcnt(0) lgkmcnt(0)\n"
      "s_barrier\n"
      "v_exp_f32 v154, v154\n"
      "v_exp_f32 v155, v155\n"
      "v_exp_f32 v156, v156\n"
      "v_exp_f32 v157, v157\n"
      "v_exp_f32 v158, v158\n"
      "v_exp_f32 v159, v159\n"
      "v_exp_f32 v160, v160\n"
      "v_exp_f32 v161, v161\n"
      "v_cvt_pk_bf16_f32 v82, v154, v155\n"
      "v_cvt_pk_bf16_f32 v83, v156, v157\n"
      "v_cvt_pk_bf16_f32 v84, v158, v159\n"
      "v_cvt_pk_bf16_f32 v85, v160, v161\n"
      "v_exp_f32 v162, v162\n"
      "v_exp_f32 v163, v163\n"
      "v_exp_f32 v164, v164\n"
      "v_exp_f32 v165, v165\n"
      "v_exp_f32 v166, v166\n"
      "v_exp_f32 v167, v167\n"
      "v_exp_f32 v168, v168\n"
      "v_exp_f32 v169, v169\n"
      "v_cvt_pk_bf16_f32 v86, v162, v163\n"
      "v_cvt_pk_bf16_f32 v87, v164, v165\n"
      "v_cvt_pk_bf16_f32 v88, v166, v167\n"
      "v_cvt_pk_bf16_f32 v89, v168, v169\n"
      "v_exp_f32 v170, v170\n"
      "v_exp_f32 v171, v171\n"
      "v_exp_f32 v172, v172\n"
      "v_exp_f32 v173, v173\n"
      "v_exp_f32 v174, v174\n"
      "v_exp_f32 v175, v175\n"
      "v_exp_f32 v176, v176\n"
      "v_exp_f32 v177, v177\n"
      "v_cvt_pk_bf16_f32 v90, v170, v171\n"
      "v_cvt_pk_bf16_f32 v91, v172, v173\n"
      "v_cvt_pk_bf16_f32 v92, v174, v175\n"
      "v_cvt_pk_bf16_f32 v93, v176, v177\n"
      "v_exp_f32 v178, v178\n"
      "v_exp_f32 v179, v179\n"
      "v_exp_f32 v180, v180\n"
      "v_exp_f32 v181, v181\n"
      "v_exp_f32 v182, v182\n"
      "v_exp_f32 v183, v183\n"
      "v_exp_f32 v184, v184\n"
      "v_exp_f32 v185, v185\n"
      "v_cvt_pk_bf16_f32 v94, v178, v179\n"
      "v_cvt_pk_bf16_f32 v95, v180, v181\n"
      "v_cvt_pk_bf16_f32 v96, v182, v183\n"
      "v_cvt_pk_bf16_f32 v97, v184, v185\n"
      "v_exp_f32 v186, v186\n"
      "v_exp_f32 v187, v187\n"
      "v_exp_f32 v188, v188\n"
      "v_exp_f32 v189, v189\n"
      "v_exp_f32 v190, v190\n"
      "v_exp_f32 v191, v191\n"
      "v_exp_f32 v192, v192\n"
      "v_exp_f32 v193, v193\n"
      "v_cvt_pk_bf16_f32 v98, v186, v187\n"
      "v_cvt_pk_bf16_f32 v99, v188, v189\n"
      "v_cvt_pk_bf16_f32 v100, v190, v191\n"
      "v_cvt_pk_bf16_f32 v101, v192, v193\n"
      "v_exp_f32 v194, v194\n"
      "v_exp_f32 v195, v195\n"
      "v_exp_f32 v196, v196\n"
      "v_exp_f32 v197, v197\n"
      "v_exp_f32 v198, v198\n"
      "v_exp_f32 v199, v199\n"
      "v_exp_f32 v200, v200\n"
      "v_exp_f32 v201, v201\n"
      "v_cvt_pk_bf16_f32 v102, v194, v195\n"
      "v_cvt_pk_bf16_f32 v103, v196, v197\n"
      "v_cvt_pk_bf16_f32 v104, v198, v199\n"
      "v_cvt_pk_bf16_f32 v105, v200, v201\n"
      "ds_read_b64_tr_b16 v[50:51], v21 offset:32768\n"
      "ds_read_b64_tr_b16 v[52:53], v21 offset:40960\n"
      "ds_read_b64_tr_b16 v[54:55], v21 offset:49152\n"
      "ds_read_b64_tr_b16 v[56:57], v21 offset:57344\n"
      "ds_read_b64_tr_b16 v[58:59], v22 offset:32768\n"
      "ds_read_b64_tr_b16 v[60:61], v22 offset:40960\n"
      "ds_read_b64_tr_b16 v[62:63], v22 offset:49152\n"
      "ds_read_b64_tr_b16 v[64:65], v22 offset:57344\n"
      "ds_read_b64_tr_b16 v[66:67], v23 offset:32768\n"
      "ds_read_b64_tr_b16 v[68:69], v23 offset:40960\n"
      "ds_read_b64_tr_b16 v[70:71], v23 offset:49152\n"
      "ds_read_b64_tr_b16 v[72:73], v23 offset:57344\n"
      "v_mfma_f32_16x16x32_bf16 a[192:195], v[34:37], v[82:85], a[192:195]\n"
      "v_mfma_f32_16x16x32_bf16 a[192:195], v[34:37], v[86:89], a[192:195]\n"
      "v_mfma_f32_16x16x32_bf16 a[196:199], v[34:37], v[90:93], a[196:199]\n"
      "v_mfma_f32_16x16x32_bf16 a[196:199], v[34:37], v[94:97], a[196:199]\n"
      "v_mfma_f32_16x16x32_bf16 a[200:203], v[34:37], v[98:101], a[200:203]\n"
      "v_mfma_f32_16x16x32_bf16 a[200:203], v[34:37], v[102:105], a[200:203]\n"
      "s_waitcnt lgkmcnt(8)\n"
      "v_mfma_f32_16x16x32_bf16 a[0:3], v[50:53], v[82:85], a[0:3]\n"
      "ds_read_b64_tr_b16 v[74:75], v24 offset:32768\n"
      "v_mfma_f32_16x16x32_bf16 a[64:67], v[50:53], v[90:93], a[64:67]\n"
      "ds_read_b64_tr_b16 v[76:77], v24 offset:40960\n"
      "v_mfma_f32_16x16x32_bf16 a[128:131], v[50:53], v[98:101], a[128:131]\n"
      "v_mfma_f32_16x16x32_bf16 a[0:3], v[54:57], v[86:89], a[0:3]\n"
      "ds_read_b64_tr_b16 v[78:79], v24 offset:49152\n"
      "v_mfma_f32_16x16x32_bf16 a[64:67], v[54:57], v[94:97], a[64:67]\n"
      "ds_read_b64_tr_b16 v[80:81], v24 offset:57344\n"
      "v_mfma_f32_16x16x32_bf16 a[128:131], v[54:57], v[102:105], a[128:131]\n"
      "s_waitcnt lgkmcnt(8)\n"
      "v_mfma_f32_16x16x32_bf16 a[4:7], v[58:61], v[82:85], a[4:7]\n"
      "ds_read_b64_tr_b16 v[50:51], v25 offset:32768\n"
      "v_mfma_f32_16x16x32_bf16 a[68:71], v[58:61], v[90:93], a[68:71]\n"
      "ds_read_b64_tr_b16 v[52:53], v25 offset:40960\n"
      "v_mfma_f32_16x16x32_bf16 a[132:135], v[58:61], v[98:101], a[132:135]\n"
      "v_mfma_f32_16x16x32_bf16 a[4:7], v[62:65], v[86:89], a[4:7]\n"
      "ds_read_b64_tr_b16 v[54:55], v25 offset:49152\n"
      "v_mfma_f32_16x16x32_bf16 a[68:71], v[62:65], v[94:97], a[68:71]\n"
      "ds_read_b64_tr_b16 v[56:57], v25 offset:57344\n"
      "v_mfma_f32_16x16x32_bf16 a[132:135], v[62:65], v[102:105], a[132:135]\n"
      "s_waitcnt lgkmcnt(8)\n"
      "v_mfma_f32_16x16x32_bf16 a[8:11], v[66:69], v[82:85], a[8:11]\n"
      "ds_read_b64_tr_b16 v[58:59], v26 offset:32768\n"
      "v_mfma_f32_16x16x32_bf16 a[72:75], v[66:69], v[90:93], a[72:75]\n"
      "ds_read_b64_tr_b16 v[60:61], v26 offset:40960\n"
      "v_mfma_f32_16x16x32_bf16 a[136:139], v[66:69], v[98:101], a[136:139]\n"
      "v_mfma_f32_16x16x32_bf16 a[8:11], v[70:73], v[86:89], a[8:11]\n"
      "ds_read_b64_tr_b16 v[62:63], v26 offset:49152\n"
      "v_mfma_f32_16x16x32_bf16 a[72:75], v[70:73], v[94:97], a[72:75]\n"
      "ds_read_b64_tr_b16 v[64:65], v26 offset:57344\n"
      "v_mfma_f32_16x16x32_bf16 a[136:139], v[70:73], v[102:105], a[136:139]\n"
      "s_waitcnt lgkmcnt(8)\n"
      "v_mfma_f32_16x16x32_bf16 a[12:15], v[74:77], v[82:85], a[12:15]\n"
      "ds_read_b64_tr_b16 v[66:67], v27 offset:32768\n"
      "v_mfma_f32_16x16x32_bf16 a[76:79], v[74:77], v[90:93], a[76:79]\n"
      "ds_read_b64_tr_b16 v[68:69], v27 offset:40960\n"
      "v_mfma_f32_16x16x32_bf16 a[140:143], v[74:77], v[98:101], a[140:143]\n"
      "v_mfma_f32_16x16x32_bf16 a[12:15], v[78:81], v[86:89], a[12:15]\n"
      "ds_read_b64_tr_b16 v[70:71], v27 offset:49152\n"
      "v_mfma_f32_16x16x32_bf16 a[76:79], v[78:81], v[94:97], a[76:79]\n"
      "ds_read_b64_tr_b16 v[72:73], v27 offset:57344\n"
      "v_mfma_f32_16x16x32_bf16 a[140:143], v[78:81], v[102:105], a[140:143]\n"
      "s_waitcnt lgkmcnt(8)\n"
      "v_mfma_f32_16x16x32_bf16 a[16:19], v[50:53], v[82:85], a[16:19]\n"
      "ds_read_b64_tr_b16 v[74:75], v28 offset:32768\n"
      "v_mfma_f32_16x16x32_bf16 a[80:83], v[50:53], v[90:93], a[80:83]\n"
      "ds_read_b64_tr_b16 v[76:77], v28 offset:40960\n"
      "v_mfma_f32_16x16x32_bf16 a[144:147], v[50:53], v[98:101], a[144:147]\n"
      "v_mfma_f32_16x16x32_bf16 a[16:19], v[54:57], v[86:89], a[16:19]\n"
      "ds_read_b64_tr_b16 v[78:79], v28 offset:49152\n"
      "v_mfma_f32_16x16x32_bf16 a[80:83], v[54:57], v[94:97], a[80:83]\n"
      "ds_read_b64_tr_b16 v[80:81], v28 offset:57344\n"
      "v_mfma_f32_16x16x32_bf16 a[144:147], v[54:57], v[102:105], a[144:147]\n"
      "s_waitcnt lgkmcnt(8)\n"
      "v_mfma_f32_16x16x32_bf16 a[20:23], v[58:61], v[82:85], a[20:23]\n"
      "ds_read_b64_tr_b16 v[50:51], v21 offset:33024\n"
      "v_mfma_f32_16x16x32_bf16 a[84:87], v[58:61], v[90:93], a[84:87]\n"
      "ds_read_b64_tr_b16 v[52:53], v21 offset:41216\n"
      "v_mfma_f32_16x16x32_bf16 a[148:151], v[58:61], v[98:101], a[148:151]\n"
      "v_mfma_f32_16x16x32_bf16 a[20:23], v[62:65], v[86:89], a[20:23]\n"
      "ds_read_b64_tr_b16 v[54:55], v21 offset:49408\n"
      "v_mfma_f32_16x16x32_bf16 a[84:87], v[62:65], v[94:97], a[84:87]\n"
      "ds_read_b64_tr_b16 v[56:57], v21 offset:57600\n"
      "v_mfma_f32_16x16x32_bf16 a[148:151], v[62:65], v[102:105], a[148:151]\n"
      "s_waitcnt lgkmcnt(8)\n"
      "v_mfma_f32_16x16x32_bf16 a[24:27], v[66:69], v[82:85], a[24:27]\n"
      "ds_read_b64_tr_b16 v[58:59], v22 offset:33024\n"
      "v_mfma_f32_16x16x32_bf16 a[88:91], v[66:69], v[90:93], a[88:91]\n"
      "ds_read_b64_tr_b16 v[60:61], v22 offset:41216\n"
      "v_mfma_f32_16x16x32_bf16 a[152:155], v[66:69], v[98:101], a[152:155]\n"
      "v_mfma_f32_16x16x32_bf16 a[24:27], v[70:73], v[86:89], a[24:27]\n"
      "ds_read_b64_tr_b16 v[62:63], v22 offset:49408\n"
      "v_mfma_f32_16x16x32_bf16 a[88:91], v[70:73], v[94:97], a[88:91]\n"
      "ds_read_b64_tr_b16 v[64:65], v22 offset:57600\n"
      "v_mfma_f32_16x16x32_bf16 a[152:155], v[70:73], v[102:105], a[152:155]\n"
      "s_waitcnt lgkmcnt(8)\n"
      "v_mfma_f32_16x16x32_bf16 a[28:31], v[74:77], v[82:85], a[28:31]\n"
      "ds_read_b64_tr_b16 v[66:67], v23 offset:33024\n"
      "v_mfma_f32_16x16x32_bf16 a[92:95], v[74:77], v[90:93], a[92:95]\n"
      "ds_read_b64_tr_b16 v[68:69], v23 offset:41216\n"
      "v_mfma_f32_16x16x32_bf16 a[156:159], v[74:77], v[98:101], a[156:159]\n"
      "v_mfma_f32_16x16x32_bf16 a[28:31], v[78:81], v[86:89], a[28:31]\n"
      "ds_read_b64_tr_b16 v[70:71], v23 offset:49408\n"
      "v_mfma_f32_16x16x32_bf16 a[92:95], v[78:81], v[94:97], a[92:95]\n"
      "ds_read_b64_tr_b16 v[72:73], v23 offset:57600\n"
      "v_mfma_f32_16x16x32_bf16 a[156:159], v[78:81], v[102:105], a[156:159]\n"
      "s_waitcnt lgkmcnt(8)\n"
      "v_mfma_f32_16x16x32_bf16 a[32:35], v[50:53], v[82:85], a[32:35]\n"
      "ds_read_b64_tr_b16 v[74:75], v24 offset:33024\n"
      "v_mfma_f32_16x16x32_bf16 a[96:99], v[50:53], v[90:93], a[96:99]\n"
      "ds_read_b64_tr_b16 v[76:77], v24 offset:41216\n"
      "v_mfma_f32_16x16x32_bf16 a[160:163], v[50:53], v[98:101], a[160:163]\n"
      "v_mfma_f32_16x16x32_bf16 a[32:35], v[54:57], v[86:89], a[32:35]\n"
      "ds_read_b64_tr_b16 v[78:79], v24 offset:49408\n"
      "v_mfma_f32_16x16x32_bf16 a[96:99], v[54:57], v[94:97], a[96:99]\n"
      "ds_read_b64_tr_b16 v[80:81], v24 offset:57600\n"
      "v_mfma_f32_16x16x32_bf16 a[160:163], v[54:57], v[102:105], a[160:163]\n"
      "s_waitcnt lgkmcnt(8)\n"
      "v_mfma_f32_16x16x32_bf16 a[36:39], v[58:61], v[82:85], a[36:39]\n"
      "ds_read_b64_tr_b16 v[50:51], v25 offset:33024\n"
      "v_mfma_f32_16x16x32_bf16 a[100:103], v[58:61], v[90:93], a[100:103]\n"
      "ds_read_b64_tr_b16 v[52:53], v25 offset:41216\n"
      "v_mfma_f32_16x16x32_bf16 a[164:167], v[58:61], v[98:101], a[164:167]\n"
      "v_mfma_f32_16x16x32_bf16 a[36:39], v[62:65], v[86:89], a[36:39]\n"
      "ds_read_b64_tr_b16 v[54:55], v25 offset:49408\n"
      "v_mfma_f32_16x16x32_bf16 a[100:103], v[62:65], v[94:97], a[100:103]\n"
      "ds_read_b64_tr_b16 v[56:57], v25 offset:57600\n"
      "v_mfma_f32_16x16x32_bf16 a[164:167], v[62:65], v[102:105], a[164:167]\n"
      "s_waitcnt lgkmcnt(8)\n"
      "v_mfma_f32_16x16x32_bf16 a[40:43], v[66:69], v[82:85], a[40:43]\n"
      "ds_read_b64_tr_b16 v[58:59], v26 offset:33024\n"
      "v_mfma_f32_16x16x32_bf16 a[104:107], v[66:69], v[90:93], a[104:107]\n"
      "ds_read_b64_tr_b16 v[60:61], v26 offset:41216\n"
      "v_mfma_f32_16x16x32_bf16 a[168:171], v[66:69], v[98:101], a[168:171]\n"
      "v_mfma_f32_16x16x32_bf16 a[40:43], v[70:73], v[86:89], a[40:43]\n"
      "ds_read_b64_tr_b16 v[62:63], v26 offset:49408\n"
      "v_mfma_f32_16x16x32_bf16 a[104:107], v[70:73], v[94:97], a[104:107]\n"
      "ds_read_b64_tr_b16 v[64:65], v26 offset:57600\n"
      "v_mfma_f32_16x16x32_bf16 a[168:171], v[70:73], v[102:105], a[168:171]\n"
      "s_waitcnt lgkmcnt(8)\n"
      "v_mfma_f32_16x16x32_bf16 a[44:47], v[74:77], v[82:85], a[44:47]\n"
      "ds_read_b64_tr_b16 v[66:67], v27 offset:33024\n"
      "v_mfma_f32_16x16x32_bf16 a[108:111], v[74:77], v[90:93], a[108:111]\n"
      "ds_read_b64_tr_b16 v[68:69], v27 offset:41216\n"
      "v_mfma_f32_16x16x32_bf16 a[172:175], v[74:77], v[98:101], a[172:175]\n"
      "v_mfma_f32_16x16x32_bf16 a[44:47], v[78:81], v[86:89], a[44:47]\n"
      "ds_read_b64_tr_b16 v[70:71], v27 offset:49408\n"
      "v_mfma_f32_16x16x32_bf16 a[108:111], v[78:81], v[94:97], a[108:111]\n"
      "ds_read_b64_tr_b16 v[72:73], v27 offset:57600\n"
      "v_mfma_f32_16x16x32_bf16 a[172:175], v[78:81], v[102:105], a[172:175]\n"
      "s_waitcnt lgkmcnt(8)\n"
      "v_mfma_f32_16x16x32_bf16 a[48:51], v[50:53], v[82:85], a[48:51]\n"
      "ds_read_b64_tr_b16 v[74:75], v28 offset:33024\n"
      "v_mfma_f32_16x16x32_bf16 a[112:115], v[50:53], v[90:93], a[112:115]\n"
      "ds_read_b64_tr_b16 v[76:77], v28 offset:41216\n"
      "v_mfma_f32_16x16x32_bf16 a[176:179], v[50:53], v[98:101], a[176:179]\n"
      "v_mfma_f32_16x16x32_bf16 a[48:51], v[54:57], v[86:89], a[48:51]\n"
      "ds_read_b64_tr_b16 v[78:79], v28 offset:49408\n"
      "v_mfma_f32_16x16x32_bf16 a[112:115], v[54:57], v[94:97], a[112:115]\n"
      "ds_read_b64_tr_b16 v[80:81], v28 offset:57600\n"
      "v_mfma_f32_16x16x32_bf16 a[176:179], v[54:57], v[102:105], a[176:179]\n"
      "s_waitcnt lgkmcnt(8)\n"
      "v_mfma_f32_16x16x32_bf16 a[52:55], v[58:61], v[82:85], a[52:55]\n"
      "v_mfma_f32_16x16x32_bf16 a[116:119], v[58:61], v[90:93], a[116:119]\n"
      "v_mfma_f32_16x16x32_bf16 a[180:183], v[58:61], v[98:101], a[180:183]\n"
      "v_mfma_f32_16x16x32_bf16 a[52:55], v[62:65], v[86:89], a[52:55]\n"
      "v_mfma_f32_16x16x32_bf16 a[116:119], v[62:65], v[94:97], a[116:119]\n"
      "v_mfma_f32_16x16x32_bf16 a[180:183], v[62:65], v[102:105], a[180:183]\n"
      "s_waitcnt lgkmcnt(4)\n"
      "v_mfma_f32_16x16x32_bf16 a[56:59], v[66:69], v[82:85], a[56:59]\n"
      "v_mfma_f32_16x16x32_bf16 a[120:123], v[66:69], v[90:93], a[120:123]\n"
      "v_mfma_f32_16x16x32_bf16 a[184:187], v[66:69], v[98:101], a[184:187]\n"
      "v_mfma_f32_16x16x32_bf16 a[56:59], v[70:73], v[86:89], a[56:59]\n"
      "v_mfma_f32_16x16x32_bf16 a[120:123], v[70:73], v[94:97], a[120:123]\n"
      "v_mfma_f32_16x16x32_bf16 a[184:187], v[70:73], v[102:105], a[184:187]\n"
      "s_waitcnt lgkmcnt(2)\n"
      "v_mfma_f32_16x16x32_bf16 a[60:63], v[74:77], v[82:85], a[60:63]\n"
      "v_mfma_f32_16x16x32_bf16 a[124:127], v[74:77], v[90:93], a[124:127]\n"
      "v_mfma_f32_16x16x32_bf16 a[188:191], v[74:77], v[98:101], a[188:191]\n"
      "s_waitcnt lgkmcnt(0)\n"
      "v_mfma_f32_16x16x32_bf16 a[60:63], v[78:81], v[86:89], a[60:63]\n"
      "v_mfma_f32_16x16x32_bf16 a[124:127], v[78:81], v[94:97], a[124:127]\n"
      "v_mfma_f32_16x16x32_bf16 a[188:191], v[78:81], v[102:105], a[188:191]\n"
      "s_waitcnt vmcnt(0) lgkmcnt(0)\n"
      "s_barrier\n"
      "s_nop 15\n"
      "s_mul_i32 s84, %4, 4\n"
      "v_mul_u32_u24 v3, 528, v1\n"
      "v_lshl_add_u32 v3, v2, 3, v3\n"
      "v_add_u32 v3, s84, v3\n"
      "v_accvgpr_read_b32 v7, a192\n"
      "s_nop 1\n"
      "v_rcp_f32 v7, v7\n"
      "s_nop 1\n"
      "v_accvgpr_read_b32 v106, a0\n"
      "v_accvgpr_read_b32 v107, a1\n"
      "v_accvgpr_read_b32 v108, a2\n"
      "v_accvgpr_read_b32 v109, a3\n"
      "v_mul_f32 v106, v106, v7\n"
      "v_mul_f32 v107, v107, v7\n"
      "v_mul_f32 v108, v108, v7\n"
      "v_mul_f32 v109, v109, v7\n"
      "v_cvt_pk_bf16_f32 v106, v106, v107\n"
      "v_cvt_pk_bf16_f32 v107, v108, v109\n"
      "ds_write_b64 v3, v[106:107] offset:0\n"
      "v_accvgpr_read_b32 v110, a4\n"
      "v_accvgpr_read_b32 v111, a5\n"
      "v_accvgpr_read_b32 v112, a6\n"
      "v_accvgpr_read_b32 v113, a7\n"
      "v_mul_f32 v110, v110, v7\n"
      "v_mul_f32 v111, v111, v7\n"
      "v_mul_f32 v112, v112, v7\n"
      "v_mul_f32 v113, v113, v7\n"
      "v_cvt_pk_bf16_f32 v110, v110, v111\n"
      "v_cvt_pk_bf16_f32 v111, v112, v113\n"
      "ds_write_b64 v3, v[110:111] offset:32\n"
      "v_accvgpr_read_b32 v114, a8\n"
      "v_accvgpr_read_b32 v115, a9\n"
      "v_accvgpr_read_b32 v116, a10\n"
      "v_accvgpr_read_b32 v117, a11\n"
      "v_mul_f32 v114, v114, v7\n"
      "v_mul_f32 v115, v115, v7\n"
      "v_mul_f32 v116, v116, v7\n"
      "v_mul_f32 v117, v117, v7\n"
      "v_cvt_pk_bf16_f32 v114, v114, v115\n"
      "v_cvt_pk_bf16_f32 v115, v116, v117\n"
      "ds_write_b64 v3, v[114:115] offset:64\n"
      "v_accvgpr_read_b32 v118, a12\n"
      "v_accvgpr_read_b32 v119, a13\n"
      "v_accvgpr_read_b32 v120, a14\n"
      "v_accvgpr_read_b32 v121, a15\n"
      "v_mul_f32 v118, v118, v7\n"
      "v_mul_f32 v119, v119, v7\n"
      "v_mul_f32 v120, v120, v7\n"
      "v_mul_f32 v121, v121, v7\n"
      "v_cvt_pk_bf16_f32 v118, v118, v119\n"
      "v_cvt_pk_bf16_f32 v119, v120, v121\n"
      "ds_write_b64 v3, v[118:119] offset:96\n"
      "v_accvgpr_read_b32 v106, a16\n"
      "v_accvgpr_read_b32 v107, a17\n"
      "v_accvgpr_read_b32 v108, a18\n"
      "v_accvgpr_read_b32 v109, a19\n"
      "v_mul_f32 v106, v106, v7\n"
      "v_mul_f32 v107, v107, v7\n"
      "v_mul_f32 v108, v108, v7\n"
      "v_mul_f32 v109, v109, v7\n"
      "v_cvt_pk_bf16_f32 v106, v106, v107\n"
      "v_cvt_pk_bf16_f32 v107, v108, v109\n"
      "ds_write_b64 v3, v[106:107] offset:128\n"
      "v_accvgpr_read_b32 v110, a20\n"
      "v_accvgpr_read_b32 v111, a21\n"
      "v_accvgpr_read_b32 v112, a22\n"
      "v_accvgpr_read_b32 v113, a23\n"
      "v_mul_f32 v110, v110, v7\n"
      "v_mul_f32 v111, v111, v7\n"
      "v_mul_f32 v112, v112, v7\n"
      "v_mul_f32 v113, v113, v7\n"
      "v_cvt_pk_bf16_f32 v110, v110, v111\n"
      "v_cvt_pk_bf16_f32 v111, v112, v113\n"
      "ds_write_b64 v3, v[110:111] offset:160\n"
      "v_accvgpr_read_b32 v114, a24\n"
      "v_accvgpr_read_b32 v115, a25\n"
      "v_accvgpr_read_b32 v116, a26\n"
      "v_accvgpr_read_b32 v117, a27\n"
      "v_mul_f32 v114, v114, v7\n"
      "v_mul_f32 v115, v115, v7\n"
      "v_mul_f32 v116, v116, v7\n"
      "v_mul_f32 v117, v117, v7\n"
      "v_cvt_pk_bf16_f32 v114, v114, v115\n"
      "v_cvt_pk_bf16_f32 v115, v116, v117\n"
      "ds_write_b64 v3, v[114:115] offset:192\n"
      "v_accvgpr_read_b32 v118, a28\n"
      "v_accvgpr_read_b32 v119, a29\n"
      "v_accvgpr_read_b32 v120, a30\n"
      "v_accvgpr_read_b32 v121, a31\n"
      "v_mul_f32 v118, v118, v7\n"
      "v_mul_f32 v119, v119, v7\n"
      "v_mul_f32 v120, v120, v7\n"
      "v_mul_f32 v121, v121, v7\n"
      "v_cvt_pk_bf16_f32 v118, v118, v119\n"
      "v_cvt_pk_bf16_f32 v119, v120, v121\n"
      "ds_write_b64 v3, v[118:119] offset:224\n"
      "v_accvgpr_read_b32 v106, a32\n"
      "v_accvgpr_read_b32 v107, a33\n"
      "v_accvgpr_read_b32 v108, a34\n"
      "v_accvgpr_read_b32 v109, a35\n"
      "v_mul_f32 v106, v106, v7\n"
      "v_mul_f32 v107, v107, v7\n"
      "v_mul_f32 v108, v108, v7\n"
      "v_mul_f32 v109, v109, v7\n"
      "v_cvt_pk_bf16_f32 v106, v106, v107\n"
      "v_cvt_pk_bf16_f32 v107, v108, v109\n"
      "ds_write_b64 v3, v[106:107] offset:256\n"
      "v_accvgpr_read_b32 v110, a36\n"
      "v_accvgpr_read_b32 v111, a37\n"
      "v_accvgpr_read_b32 v112, a38\n"
      "v_accvgpr_read_b32 v113, a39\n"
      "v_mul_f32 v110, v110, v7\n"
      "v_mul_f32 v111, v111, v7\n"
      "v_mul_f32 v112, v112, v7\n"
      "v_mul_f32 v113, v113, v7\n"
      "v_cvt_pk_bf16_f32 v110, v110, v111\n"
      "v_cvt_pk_bf16_f32 v111, v112, v113\n"
      "ds_write_b64 v3, v[110:111] offset:288\n"
      "v_accvgpr_read_b32 v114, a40\n"
      "v_accvgpr_read_b32 v115, a41\n"
      "v_accvgpr_read_b32 v116, a42\n"
      "v_accvgpr_read_b32 v117, a43\n"
      "v_mul_f32 v114, v114, v7\n"
      "v_mul_f32 v115, v115, v7\n"
      "v_mul_f32 v116, v116, v7\n"
      "v_mul_f32 v117, v117, v7\n"
      "v_cvt_pk_bf16_f32 v114, v114, v115\n"
      "v_cvt_pk_bf16_f32 v115, v116, v117\n"
      "ds_write_b64 v3, v[114:115] offset:320\n"
      "v_accvgpr_read_b32 v118, a44\n"
      "v_accvgpr_read_b32 v119, a45\n"
      "v_accvgpr_read_b32 v120, a46\n"
      "v_accvgpr_read_b32 v121, a47\n"
      "v_mul_f32 v118, v118, v7\n"
      "v_mul_f32 v119, v119, v7\n"
      "v_mul_f32 v120, v120, v7\n"
      "v_mul_f32 v121, v121, v7\n"
      "v_cvt_pk_bf16_f32 v118, v118, v119\n"
      "v_cvt_pk_bf16_f32 v119, v120, v121\n"
      "ds_write_b64 v3, v[118:119] offset:352\n"
      "v_accvgpr_read_b32 v106, a48\n"
      "v_accvgpr_read_b32 v107, a49\n"
      "v_accvgpr_read_b32 v108, a50\n"
      "v_accvgpr_read_b32 v109, a51\n"
      "v_mul_f32 v106, v106, v7\n"
      "v_mul_f32 v107, v107, v7\n"
      "v_mul_f32 v108, v108, v7\n"
      "v_mul_f32 v109, v109, v7\n"
      "v_cvt_pk_bf16_f32 v106, v106, v107\n"
      "v_cvt_pk_bf16_f32 v107, v108, v109\n"
      "ds_write_b64 v3, v[106:107] offset:384\n"
      "v_accvgpr_read_b32 v110, a52\n"
      "v_accvgpr_read_b32 v111, a53\n"
      "v_accvgpr_read_b32 v112, a54\n"
      "v_accvgpr_read_b32 v113, a55\n"
      "v_mul_f32 v110, v110, v7\n"
      "v_mul_f32 v111, v111, v7\n"
      "v_mul_f32 v112, v112, v7\n"
      "v_mul_f32 v113, v113, v7\n"
      "v_cvt_pk_bf16_f32 v110, v110, v111\n"
      "v_cvt_pk_bf16_f32 v111, v112, v113\n"
      "ds_write_b64 v3, v[110:111] offset:416\n"
      "v_accvgpr_read_b32 v114, a56\n"
      "v_accvgpr_read_b32 v115, a57\n"
      "v_accvgpr_read_b32 v116, a58\n"
      "v_accvgpr_read_b32 v117, a59\n"
      "v_mul_f32 v114, v114, v7\n"
      "v_mul_f32 v115, v115, v7\n"
      "v_mul_f32 v116, v116, v7\n"
      "v_mul_f32 v117, v117, v7\n"
      "v_cvt_pk_bf16_f32 v114, v114, v115\n"
      "v_cvt_pk_bf16_f32 v115, v116, v117\n"
      "ds_write_b64 v3, v[114:115] offset:448\n"
      "v_accvgpr_read_b32 v118, a60\n"
      "v_accvgpr_read_b32 v119, a61\n"
      "v_accvgpr_read_b32 v120, a62\n"
      "v_accvgpr_read_b32 v121, a63\n"
      "v_mul_f32 v118, v118, v7\n"
      "v_mul_f32 v119, v119, v7\n"
      "v_mul_f32 v120, v120, v7\n"
      "v_mul_f32 v121, v121, v7\n"
      "v_cvt_pk_bf16_f32 v118, v118, v119\n"
      "v_cvt_pk_bf16_f32 v119, v120, v121\n"
      "ds_write_b64 v3, v[118:119] offset:480\n"
      "v_accvgpr_read_b32 v7, a196\n"
      "s_nop 1\n"
      "v_rcp_f32 v7, v7\n"
      "s_nop 1\n"
      "v_accvgpr_read_b32 v106, a64\n"
      "v_accvgpr_read_b32 v107, a65\n"
      "v_accvgpr_read_b32 v108, a66\n"
      "v_accvgpr_read_b32 v109, a67\n"
      "v_mul_f32 v106, v106, v7\n"
      "v_mul_f32 v107, v107, v7\n"
      "v_mul_f32 v108, v108, v7\n"
      "v_mul_f32 v109, v109, v7\n"
      "v_cvt_pk_bf16_f32 v106, v106, v107\n"
      "v_cvt_pk_bf16_f32 v107, v108, v109\n"
      "ds_write_b64 v3, v[106:107] offset:8448\n"
      "v_accvgpr_read_b32 v110, a68\n"
      "v_accvgpr_read_b32 v111, a69\n"
      "v_accvgpr_read_b32 v112, a70\n"
      "v_accvgpr_read_b32 v113, a71\n"
      "v_mul_f32 v110, v110, v7\n"
      "v_mul_f32 v111, v111, v7\n"
      "v_mul_f32 v112, v112, v7\n"
      "v_mul_f32 v113, v113, v7\n"
      "v_cvt_pk_bf16_f32 v110, v110, v111\n"
      "v_cvt_pk_bf16_f32 v111, v112, v113\n"
      "ds_write_b64 v3, v[110:111] offset:8480\n"
      "v_accvgpr_read_b32 v114, a72\n"
      "v_accvgpr_read_b32 v115, a73\n"
      "v_accvgpr_read_b32 v116, a74\n"
      "v_accvgpr_read_b32 v117, a75\n"
      "v_mul_f32 v114, v114, v7\n"
      "v_mul_f32 v115, v115, v7\n"
      "v_mul_f32 v116, v116, v7\n"
      "v_mul_f32 v117, v117, v7\n"
      "v_cvt_pk_bf16_f32 v114, v114, v115\n"
      "v_cvt_pk_bf16_f32 v115, v116, v117\n"
      "ds_write_b64 v3, v[114:115] offset:8512\n"
      "v_accvgpr_read_b32 v118, a76\n"
      "v_accvgpr_read_b32 v119, a77\n"
      "v_accvgpr_read_b32 v120, a78\n"
      "v_accvgpr_read_b32 v121, a79\n"
      "v_mul_f32 v118, v118, v7\n"
      "v_mul_f32 v119, v119, v7\n"
      "v_mul_f32 v120, v120, v7\n"
      "v_mul_f32 v121, v121, v7\n"
      "v_cvt_pk_bf16_f32 v118, v118, v119\n"
      "v_cvt_pk_bf16_f32 v119, v120, v121\n"
      "ds_write_b64 v3, v[118:119] offset:8544\n"
      "v_accvgpr_read_b32 v106, a80\n"
      "v_accvgpr_read_b32 v107, a81\n"
      "v_accvgpr_read_b32 v108, a82\n"
      "v_accvgpr_read_b32 v109, a83\n"
      "v_mul_f32 v106, v106, v7\n"
      "v_mul_f32 v107, v107, v7\n"
      "v_mul_f32 v108, v108, v7\n"
      "v_mul_f32 v109, v109, v7\n"
      "v_cvt_pk_bf16_f32 v106, v106, v107\n"
      "v_cvt_pk_bf16_f32 v107, v108, v109\n"
      "ds_write_b64 v3, v[106:107] offset:8576\n"
      "v_accvgpr_read_b32 v110, a84\n"
      "v_accvgpr_read_b32 v111, a85\n"
      "v_accvgpr_read_b32 v112, a86\n"
      "v_accvgpr_read_b32 v113, a87\n"
      "v_mul_f32 v110, v110, v7\n"
      "v_mul_f32 v111, v111, v7\n"
      "v_mul_f32 v112, v112, v7\n"
      "v_mul_f32 v113, v113, v7\n"
      "v_cvt_pk_bf16_f32 v110, v110, v111\n"
      "v_cvt_pk_bf16_f32 v111, v112, v113\n"
      "ds_write_b64 v3, v[110:111] offset:8608\n"
      "v_accvgpr_read_b32 v114, a88\n"
      "v_accvgpr_read_b32 v115, a89\n"
      "v_accvgpr_read_b32 v116, a90\n"
      "v_accvgpr_read_b32 v117, a91\n"
      "v_mul_f32 v114, v114, v7\n"
      "v_mul_f32 v115, v115, v7\n"
      "v_mul_f32 v116, v116, v7\n"
      "v_mul_f32 v117, v117, v7\n"
      "v_cvt_pk_bf16_f32 v114, v114, v115\n"
      "v_cvt_pk_bf16_f32 v115, v116, v117\n"
      "ds_write_b64 v3, v[114:115] offset:8640\n"
      "v_accvgpr_read_b32 v118, a92\n"
      "v_accvgpr_read_b32 v119, a93\n"
      "v_accvgpr_read_b32 v120, a94\n"
      "v_accvgpr_read_b32 v121, a95\n"
      "v_mul_f32 v118, v118, v7\n"
      "v_mul_f32 v119, v119, v7\n"
      "v_mul_f32 v120, v120, v7\n"
      "v_mul_f32 v121, v121, v7\n"
      "v_cvt_pk_bf16_f32 v118, v118, v119\n"
      "v_cvt_pk_bf16_f32 v119, v120, v121\n"
      "ds_write_b64 v3, v[118:119] offset:8672\n"
      "v_accvgpr_read_b32 v106, a96\n"
      "v_accvgpr_read_b32 v107, a97\n"
      "v_accvgpr_read_b32 v108, a98\n"
      "v_accvgpr_read_b32 v109, a99\n"
      "v_mul_f32 v106, v106, v7\n"
      "v_mul_f32 v107, v107, v7\n"
      "v_mul_f32 v108, v108, v7\n"
      "v_mul_f32 v109, v109, v7\n"
      "v_cvt_pk_bf16_f32 v106, v106, v107\n"
      "v_cvt_pk_bf16_f32 v107, v108, v109\n"
      "ds_write_b64 v3, v[106:107] offset:8704\n"
      "v_accvgpr_read_b32 v110, a100\n"
      "v_accvgpr_read_b32 v111, a101\n"
      "v_accvgpr_read_b32 v112, a102\n"
      "v_accvgpr_read_b32 v113, a103\n"
      "v_mul_f32 v110, v110, v7\n"
      "v_mul_f32 v111, v111, v7\n"
      "v_mul_f32 v112, v112, v7\n"
      "v_mul_f32 v113, v113, v7\n"
      "v_cvt_pk_bf16_f32 v110, v110, v111\n"
      "v_cvt_pk_bf16_f32 v111, v112, v113\n"
      "ds_write_b64 v3, v[110:111] offset:8736\n"
      "v_accvgpr_read_b32 v114, a104\n"
      "v_accvgpr_read_b32 v115, a105\n"
      "v_accvgpr_read_b32 v116, a106\n"
      "v_accvgpr_read_b32 v117, a107\n"
      "v_mul_f32 v114, v114, v7\n"
      "v_mul_f32 v115, v115, v7\n"
      "v_mul_f32 v116, v116, v7\n"
      "v_mul_f32 v117, v117, v7\n"
      "v_cvt_pk_bf16_f32 v114, v114, v115\n"
      "v_cvt_pk_bf16_f32 v115, v116, v117\n"
      "ds_write_b64 v3, v[114:115] offset:8768\n"
      "v_accvgpr_read_b32 v118, a108\n"
      "v_accvgpr_read_b32 v119, a109\n"
      "v_accvgpr_read_b32 v120, a110\n"
      "v_accvgpr_read_b32 v121, a111\n"
      "v_mul_f32 v118, v118, v7\n"
      "v_mul_f32 v119, v119, v7\n"
      "v_mul_f32 v120, v120, v7\n"
      "v_mul_f32 v121, v121, v7\n"
      "v_cvt_pk_bf16_f32 v118, v118, v119\n"
      "v_cvt_pk_bf16_f32 v119, v120, v121\n"
      "ds_write_b64 v3, v[118:119] offset:8800\n"
      "v_accvgpr_read_b32 v106, a112\n"
      "v_accvgpr_read_b32 v107, a113\n"
      "v_accvgpr_read_b32 v108, a114\n"
      "v_accvgpr_read_b32 v109, a115\n"
      "v_mul_f32 v106, v106, v7\n"
      "v_mul_f32 v107, v107, v7\n"
      "v_mul_f32 v108, v108, v7\n"
      "v_mul_f32 v109, v109, v7\n"
      "v_cvt_pk_bf16_f32 v106, v106, v107\n"
      "v_cvt_pk_bf16_f32 v107, v108, v109\n"
      "ds_write_b64 v3, v[106:107] offset:8832\n"
      "v_accvgpr_read_b32 v110, a116\n"
      "v_accvgpr_read_b32 v111, a117\n"
      "v_accvgpr_read_b32 v112, a118\n"
      "v_accvgpr_read_b32 v113, a119\n"
      "v_mul_f32 v110, v110, v7\n"
      "v_mul_f32 v111, v111, v7\n"
      "v_mul_f32 v112, v112, v7\n"
      "v_mul_f32 v113, v113, v7\n"
      "v_cvt_pk_bf16_f32 v110, v110, v111\n"
      "v_cvt_pk_bf16_f32 v111, v112, v113\n"
      "ds_write_b64 v3, v[110:111] offset:8864\n"
      "v_accvgpr_read_b32 v114, a120\n"
      "v_accvgpr_read_b32 v115, a121\n"
      "v_accvgpr_read_b32 v116, a122\n"
      "v_accvgpr_read_b32 v117, a123\n"
      "v_mul_f32 v114, v114, v7\n"
      "v_mul_f32 v115, v115, v7\n"
      "v_mul_f32 v116, v116, v7\n"
      "v_mul_f32 v117, v117, v7\n"
      "v_cvt_pk_bf16_f32 v114, v114, v115\n"
      "v_cvt_pk_bf16_f32 v115, v116, v117\n"
      "ds_write_b64 v3, v[114:115] offset:8896\n"
      "v_accvgpr_read_b32 v118, a124\n"
      "v_accvgpr_read_b32 v119, a125\n"
      "v_accvgpr_read_b32 v120, a126\n"
      "v_accvgpr_read_b32 v121, a127\n"
      "v_mul_f32 v118, v118, v7\n"
      "v_mul_f32 v119, v119, v7\n"
      "v_mul_f32 v120, v120, v7\n"
      "v_mul_f32 v121, v121, v7\n"
      "v_cvt_pk_bf16_f32 v118, v118, v119\n"
      "v_cvt_pk_bf16_f32 v119, v120, v121\n"
      "ds_write_b64 v3, v[118:119] offset:8928\n"
      "v_accvgpr_read_b32 v7, a200\n"
      "s_nop 1\n"
      "v_rcp_f32 v7, v7\n"
      "s_nop 1\n"
      "v_accvgpr_read_b32 v106, a128\n"
      "v_accvgpr_read_b32 v107, a129\n"
      "v_accvgpr_read_b32 v108, a130\n"
      "v_accvgpr_read_b32 v109, a131\n"
      "v_mul_f32 v106, v106, v7\n"
      "v_mul_f32 v107, v107, v7\n"
      "v_mul_f32 v108, v108, v7\n"
      "v_mul_f32 v109, v109, v7\n"
      "v_cvt_pk_bf16_f32 v106, v106, v107\n"
      "v_cvt_pk_bf16_f32 v107, v108, v109\n"
      "ds_write_b64 v3, v[106:107] offset:16896\n"
      "v_accvgpr_read_b32 v110, a132\n"
      "v_accvgpr_read_b32 v111, a133\n"
      "v_accvgpr_read_b32 v112, a134\n"
      "v_accvgpr_read_b32 v113, a135\n"
      "v_mul_f32 v110, v110, v7\n"
      "v_mul_f32 v111, v111, v7\n"
      "v_mul_f32 v112, v112, v7\n"
      "v_mul_f32 v113, v113, v7\n"
      "v_cvt_pk_bf16_f32 v110, v110, v111\n"
      "v_cvt_pk_bf16_f32 v111, v112, v113\n"
      "ds_write_b64 v3, v[110:111] offset:16928\n"
      "v_accvgpr_read_b32 v114, a136\n"
      "v_accvgpr_read_b32 v115, a137\n"
      "v_accvgpr_read_b32 v116, a138\n"
      "v_accvgpr_read_b32 v117, a139\n"
      "v_mul_f32 v114, v114, v7\n"
      "v_mul_f32 v115, v115, v7\n"
      "v_mul_f32 v116, v116, v7\n"
      "v_mul_f32 v117, v117, v7\n"
      "v_cvt_pk_bf16_f32 v114, v114, v115\n"
      "v_cvt_pk_bf16_f32 v115, v116, v117\n"
      "ds_write_b64 v3, v[114:115] offset:16960\n"
      "v_accvgpr_read_b32 v118, a140\n"
      "v_accvgpr_read_b32 v119, a141\n"
      "v_accvgpr_read_b32 v120, a142\n"
      "v_accvgpr_read_b32 v121, a143\n"
      "v_mul_f32 v118, v118, v7\n"
      "v_mul_f32 v119, v119, v7\n"
      "v_mul_f32 v120, v120, v7\n"
      "v_mul_f32 v121, v121, v7\n"
      "v_cvt_pk_bf16_f32 v118, v118, v119\n"
      "v_cvt_pk_bf16_f32 v119, v120, v121\n"
      "ds_write_b64 v3, v[118:119] offset:16992\n"
      "v_accvgpr_read_b32 v106, a144\n"
      "v_accvgpr_read_b32 v107, a145\n"
      "v_accvgpr_read_b32 v108, a146\n"
      "v_accvgpr_read_b32 v109, a147\n"
      "v_mul_f32 v106, v106, v7\n"
      "v_mul_f32 v107, v107, v7\n"
      "v_mul_f32 v108, v108, v7\n"
      "v_mul_f32 v109, v109, v7\n"
      "v_cvt_pk_bf16_f32 v106, v106, v107\n"
      "v_cvt_pk_bf16_f32 v107, v108, v109\n"
      "ds_write_b64 v3, v[106:107] offset:17024\n"
      "v_accvgpr_read_b32 v110, a148\n"
      "v_accvgpr_read_b32 v111, a149\n"
      "v_accvgpr_read_b32 v112, a150\n"
      "v_accvgpr_read_b32 v113, a151\n"
      "v_mul_f32 v110, v110, v7\n"
      "v_mul_f32 v111, v111, v7\n"
      "v_mul_f32 v112, v112, v7\n"
      "v_mul_f32 v113, v113, v7\n"
      "v_cvt_pk_bf16_f32 v110, v110, v111\n"
      "v_cvt_pk_bf16_f32 v111, v112, v113\n"
      "ds_write_b64 v3, v[110:111] offset:17056\n"
      "v_accvgpr_read_b32 v114, a152\n"
      "v_accvgpr_read_b32 v115, a153\n"
      "v_accvgpr_read_b32 v116, a154\n"
      "v_accvgpr_read_b32 v117, a155\n"
      "v_mul_f32 v114, v114, v7\n"
      "v_mul_f32 v115, v115, v7\n"
      "v_mul_f32 v116, v116, v7\n"
      "v_mul_f32 v117, v117, v7\n"
      "v_cvt_pk_bf16_f32 v114, v114, v115\n"
      "v_cvt_pk_bf16_f32 v115, v116, v117\n"
      "ds_write_b64 v3, v[114:115] offset:17088\n"
      "v_accvgpr_read_b32 v118, a156\n"
      "v_accvgpr_read_b32 v119, a157\n"
      "v_accvgpr_read_b32 v120, a158\n"
      "v_accvgpr_read_b32 v121, a159\n"
      "v_mul_f32 v118, v118, v7\n"
      "v_mul_f32 v119, v119, v7\n"
      "v_mul_f32 v120, v120, v7\n"
      "v_mul_f32 v121, v121, v7\n"
      "v_cvt_pk_bf16_f32 v118, v118, v119\n"
      "v_cvt_pk_bf16_f32 v119, v120, v121\n"
      "ds_write_b64 v3, v[118:119] offset:17120\n"
      "v_accvgpr_read_b32 v106, a160\n"
      "v_accvgpr_read_b32 v107, a161\n"
      "v_accvgpr_read_b32 v108, a162\n"
      "v_accvgpr_read_b32 v109, a163\n"
      "v_mul_f32 v106, v106, v7\n"
      "v_mul_f32 v107, v107, v7\n"
      "v_mul_f32 v108, v108, v7\n"
      "v_mul_f32 v109, v109, v7\n"
      "v_cvt_pk_bf16_f32 v106, v106, v107\n"
      "v_cvt_pk_bf16_f32 v107, v108, v109\n"
      "ds_write_b64 v3, v[106:107] offset:17152\n"
      "v_accvgpr_read_b32 v110, a164\n"
      "v_accvgpr_read_b32 v111, a165\n"
      "v_accvgpr_read_b32 v112, a166\n"
      "v_accvgpr_read_b32 v113, a167\n"
      "v_mul_f32 v110, v110, v7\n"
      "v_mul_f32 v111, v111, v7\n"
      "v_mul_f32 v112, v112, v7\n"
      "v_mul_f32 v113, v113, v7\n"
      "v_cvt_pk_bf16_f32 v110, v110, v111\n"
      "v_cvt_pk_bf16_f32 v111, v112, v113\n"
      "ds_write_b64 v3, v[110:111] offset:17184\n"
      "v_accvgpr_read_b32 v114, a168\n"
      "v_accvgpr_read_b32 v115, a169\n"
      "v_accvgpr_read_b32 v116, a170\n"
      "v_accvgpr_read_b32 v117, a171\n"
      "v_mul_f32 v114, v114, v7\n"
      "v_mul_f32 v115, v115, v7\n"
      "v_mul_f32 v116, v116, v7\n"
      "v_mul_f32 v117, v117, v7\n"
      "v_cvt_pk_bf16_f32 v114, v114, v115\n"
      "v_cvt_pk_bf16_f32 v115, v116, v117\n"
      "ds_write_b64 v3, v[114:115] offset:17216\n"
      "v_accvgpr_read_b32 v118, a172\n"
      "v_accvgpr_read_b32 v119, a173\n"
      "v_accvgpr_read_b32 v120, a174\n"
      "v_accvgpr_read_b32 v121, a175\n"
      "v_mul_f32 v118, v118, v7\n"
      "v_mul_f32 v119, v119, v7\n"
      "v_mul_f32 v120, v120, v7\n"
      "v_mul_f32 v121, v121, v7\n"
      "v_cvt_pk_bf16_f32 v118, v118, v119\n"
      "v_cvt_pk_bf16_f32 v119, v120, v121\n"
      "ds_write_b64 v3, v[118:119] offset:17248\n"
      "v_accvgpr_read_b32 v106, a176\n"
      "v_accvgpr_read_b32 v107, a177\n"
      "v_accvgpr_read_b32 v108, a178\n"
      "v_accvgpr_read_b32 v109, a179\n"
      "v_mul_f32 v106, v106, v7\n"
      "v_mul_f32 v107, v107, v7\n"
      "v_mul_f32 v108, v108, v7\n"
      "v_mul_f32 v109, v109, v7\n"
      "v_cvt_pk_bf16_f32 v106, v106, v107\n"
      "v_cvt_pk_bf16_f32 v107, v108, v109\n"
      "ds_write_b64 v3, v[106:107] offset:17280\n"
      "v_accvgpr_read_b32 v110, a180\n"
      "v_accvgpr_read_b32 v111, a181\n"
      "v_accvgpr_read_b32 v112, a182\n"
      "v_accvgpr_read_b32 v113, a183\n"
      "v_mul_f32 v110, v110, v7\n"
      "v_mul_f32 v111, v111, v7\n"
      "v_mul_f32 v112, v112, v7\n"
      "v_mul_f32 v113, v113, v7\n"
      "v_cvt_pk_bf16_f32 v110, v110, v111\n"
      "v_cvt_pk_bf16_f32 v111, v112, v113\n"
      "ds_write_b64 v3, v[110:111] offset:17312\n"
      "v_accvgpr_read_b32 v114, a184\n"
      "v_accvgpr_read_b32 v115, a185\n"
      "v_accvgpr_read_b32 v116, a186\n"
      "v_accvgpr_read_b32 v117, a187\n"
      "v_mul_f32 v114, v114, v7\n"
      "v_mul_f32 v115, v115, v7\n"
      "v_mul_f32 v116, v116, v7\n"
      "v_mul_f32 v117, v117, v7\n"
      "v_cvt_pk_bf16_f32 v114, v114, v115\n"
      "v_cvt_pk_bf16_f32 v115, v116, v117\n"
      "ds_write_b64 v3, v[114:115] offset:17344\n"
      "v_accvgpr_read_b32 v118, a188\n"
      "v_accvgpr_read_b32 v119, a189\n"
      "v_accvgpr_read_b32 v120, a190\n"
      "v_accvgpr_read_b32 v121, a191\n"
      "v_mul_f32 v118, v118, v7\n"
      "v_mul_f32 v119, v119, v7\n"
      "v_mul_f32 v120, v120, v7\n"
      "v_mul_f32 v121, v121, v7\n"
      "v_cvt_pk_bf16_f32 v118, v118, v119\n"
      "v_cvt_pk_bf16_f32 v119, v120, v121\n"
      "ds_write_b64 v3, v[118:119] offset:17376\n"
      "s_waitcnt lgkmcnt(0)\n"
      "v_lshrrev_b32 v4, 5, v0\n"
      "v_and_b32 v5, 31, v0\n"
      "v_mul_u32_u24 v6, 528, v4\n"
      "v_lshl_add_u32 v6, v5, 4, v6\n"
      "v_add_u32 v6, s84, v6\n"
      "v_lshlrev_b32 v4, 9, v4\n"
      "v_lshl_add_u32 v4, v5, 4, v4\n"
      "ds_read_b128 v[106:109], v6 offset:0\n"
      "ds_read_b128 v[110:113], v6 offset:1056\n"
      "ds_read_b128 v[114:117], v6 offset:2112\n"
      "ds_read_b128 v[118:121], v6 offset:3168\n"
      "ds_read_b128 v[122:125], v6 offset:4224\n"
      "ds_read_b128 v[126:129], v6 offset:5280\n"
      "ds_read_b128 v[130:133], v6 offset:6336\n"
      "ds_read_b128 v[134:137], v6 offset:7392\n"
      "ds_read_b128 v[138:141], v6 offset:8448\n"
      "ds_read_b128 v[142:145], v6 offset:9504\n"
      "ds_read_b128 v[146:149], v6 offset:10560\n"
      "ds_read_b128 v[150:153], v6 offset:11616\n"
      "ds_read_b128 v[154:157], v6 offset:12672\n"
      "ds_read_b128 v[158:161], v6 offset:13728\n"
      "ds_read_b128 v[162:165], v6 offset:14784\n"
      "ds_read_b128 v[166:169], v6 offset:15840\n"
      "ds_read_b128 v[170:173], v6 offset:16896\n"
      "ds_read_b128 v[174:177], v6 offset:17952\n"
      "ds_read_b128 v[178:181], v6 offset:19008\n"
      "ds_read_b128 v[182:185], v6 offset:20064\n"
      "ds_read_b128 v[186:189], v6 offset:21120\n"
      "ds_read_b128 v[190:193], v6 offset:22176\n"
      "ds_read_b128 v[194:197], v6 offset:23232\n"
      "ds_read_b128 v[198:201], v6 offset:24288\n"
      "s_waitcnt lgkmcnt(0)\n"
      "global_store_dwordx4 v4, v[106:109], %3 offset:0\n"
      "global_store_dwordx4 v4, v[110:113], %3 offset:1024\n"
      "global_store_dwordx4 v4, v[114:117], %3 offset:2048\n"
      "global_store_dwordx4 v4, v[118:121], %3 offset:3072\n"
      "v_add_u32 v5, 4096, v4\n"
      "global_store_dwordx4 v5, v[122:125], %3\n"
      "v_add_u32 v5, 5120, v4\n"
      "global_store_dwordx4 v5, v[126:129], %3\n"
      "v_add_u32 v5, 6144, v4\n"
      "global_store_dwordx4 v5, v[130:133], %3\n"
      "v_add_u32 v5, 7168, v4\n"
      "global_store_dwordx4 v5, v[134:137], %3\n"
      "v_add_u32 v5, 8192, v4\n"
      "global_store_dwordx4 v5, v[138:141], %3\n"
      "v_add_u32 v5, 9216, v4\n"
      "global_store_dwordx4 v5, v[142:145], %3\n"
      "v_add_u32 v5, 10240, v4\n"
      "global_store_dwordx4 v5, v[146:149], %3\n"
      "v_add_u32 v5, 11264, v4\n"
      "global_store_dwordx4 v5, v[150:153], %3\n"
      "v_add_u32 v5, 12288, v4\n"
      "global_store_dwordx4 v5, v[154:157], %3\n"
      "v_add_u32 v5, 13312, v4\n"
      "global_store_dwordx4 v5, v[158:161], %3\n"
      "v_add_u32 v5, 14336, v4\n"
      "global_store_dwordx4 v5, v[162:165], %3\n"
      "v_add_u32 v5, 15360, v4\n"
      "global_store_dwordx4 v5, v[166:169], %3\n"
      "v_add_u32 v5, 16384, v4\n"
      "global_store_dwordx4 v5, v[170:173], %3\n"
      "v_add_u32 v5, 17408, v4\n"
      "global_store_dwordx4 v5, v[174:177], %3\n"
      "v_add_u32 v5, 18432, v4\n"
      "global_store_dwordx4 v5, v[178:181], %3\n"
      "v_add_u32 v5, 19456, v4\n"
      "global_store_dwordx4 v5, v[182:185], %3\n"
      "v_add_u32 v5, 20480, v4\n"
      "global_store_dwordx4 v5, v[186:189], %3\n"
      "v_add_u32 v5, 21504, v4\n"
      "global_store_dwordx4 v5, v[190:193], %3\n"
      "v_add_u32 v5, 22528, v4\n"
      "global_store_dwordx4 v5, v[194:197], %3\n"
      "v_add_u32 v5, 23552, v4\n"
      "global_store_dwordx4 v5, v[198:201], %3\n"
      "s_waitcnt vmcnt(0)\n"
      "s_branch END_%=\n"
      "SLOW0_%=:\n"
      "s_nop 15\n"
      "v_max_f32 v6, v29, v32\n"
      "v_sub_f32 v38, v38, v6\n"
      "v_sub_f32 v39, v39, v6\n"
      "v_sub_f32 v40, v40, v6\n"
      "v_sub_f32 v41, v41, v6\n"
      "v_sub_f32 v106, v106, v6\n"
      "v_sub_f32 v107, v107, v6\n"
      "v_sub_f32 v108, v108, v6\n"
      "v_sub_f32 v109, v109, v6\n"
      "v_sub_f32 v110, v110, v6\n"
      "v_sub_f32 v111, v111, v6\n"
      "v_sub_f32 v112, v112, v6\n"
      "v_sub_f32 v113, v113, v6\n"
      "v_sub_f32 v114, v114, v6\n"
      "v_sub_f32 v115, v115, v6\n"
      "v_sub_f32 v116, v116, v6\n"
      "v_sub_f32 v117, v117, v6\n"
      "v_sub_f32 v118, v118, v6\n"
      "v_sub_f32 v119, v119, v6\n"
      "v_sub_f32 v120, v120, v6\n"
      "v_sub_f32 v121, v121, v6\n"
      "v_exp_f32 v7, -v6\n"
      "v_max_f32 v6, v30, v32\n"
      "v_sub_f32 v42, v42, v6\n"
      "v_sub_f32 v43, v43, v6\n"
      "v_sub_f32 v44, v44, v6\n"
      "v_sub_f32 v45, v45, v6\n"
      "v_sub_f32 v122, v122, v6\n"
      "v_sub_f32 v123, v123, v6\n"
      "v_sub_f32 v124, v124, v6\n"
      "v_sub_f32 v125, v125, v6\n"
      "v_sub_f32 v126, v126, v6\n"
      "v_sub_f32 v127, v127, v6\n"
      "v_sub_f32 v128, v128, v6\n"
      "v_sub_f32 v129, v129, v6\n"
      "v_sub_f32 v130, v130, v6\n"
      "v_sub_f32 v131, v131, v6\n"
      "v_sub_f32 v132, v132, v6\n"
      "v_sub_f32 v133, v133, v6\n"
      "v_sub_f32 v134, v134, v6\n"
      "v_sub_f32 v135, v135, v6\n"
      "v_sub_f32 v136, v136, v6\n"
      "v_sub_f32 v137, v137, v6\n"
      "v_exp_f32 v7, -v6\n"
      "v_max_f32 v6, v31, v32\n"
      "v_sub_f32 v46, v46, v6\n"
      "v_sub_f32 v47, v47, v6\n"
      "v_sub_f32 v48, v48, v6\n"
      "v_sub_f32 v49, v49, v6\n"
      "v_sub_f32 v138, v138, v6\n"
      "v_sub_f32 v139, v139, v6\n"
      "v_sub_f32 v140, v140, v6\n"
      "v_sub_f32 v141, v141, v6\n"
      "v_sub_f32 v142, v142, v6\n"
      "v_sub_f32 v143, v143, v6\n"
      "v_sub_f32 v144, v144, v6\n"
      "v_sub_f32 v145, v145, v6\n"
      "v_sub_f32 v146, v146, v6\n"
      "v_sub_f32 v147, v147, v6\n"
      "v_sub_f32 v148, v148, v6\n"
      "v_sub_f32 v149, v149, v6\n"
      "v_sub_f32 v150, v150, v6\n"
      "v_sub_f32 v151, v151, v6\n"
      "v_sub_f32 v152, v152, v6\n"
      "v_sub_f32 v153, v153, v6\n"
      "v_exp_f32 v7, -v6\n"
      "v_mov_b32 v32, 0\n"
      "s_nop 3\n"
      "s_branch BACK0_%=\n"
      "SLOW1_%=:\n"
      "s_nop 15\n"
      "v_max_f32 v6, v29, v32\n"
      "v_sub_f32 v38, v38, v6\n"
      "v_sub_f32 v39, v39, v6\n"
      "v_sub_f32 v40, v40, v6\n"
      "v_sub_f32 v41, v41, v6\n"
      "v_sub_f32 v154, v154, v6\n"
      "v_sub_f32 v155, v155, v6\n"
      "v_sub_f32 v156, v156, v6\n"
      "v_sub_f32 v157, v157, v6\n"
      "v_sub_f32 v158, v158, v6\n"
      "v_sub_f32 v159, v159, v6\n"
      "v_sub_f32 v160, v160, v6\n"
      "v_sub_f32 v161, v161, v6\n"
      "v_sub_f32 v162, v162, v6\n"
      "v_sub_f32 v163, v163, v6\n"
      "v_sub_f32 v164, v164, v6\n"
      "v_sub_f32 v165, v165, v6\n"
      "v_sub_f32 v166, v166, v6\n"
      "v_sub_f32 v167, v167, v6\n"
      "v_sub_f32 v168, v168, v6\n"
      "v_sub_f32 v169, v169, v6\n"
      "v_exp_f32 v7, -v6\n"
      "v_accvgpr_read_b32 v8, a0\n"
      "v_mul_f32 v8, v8, v7\n"
      "v_accvgpr_write_b32 a0, v8\n"
      "v_accvgpr_read_b32 v9, a1\n"
      "v_mul_f32 v9, v9, v7\n"
      "v_accvgpr_write_b32 a1, v9\n"
      "v_accvgpr_read_b32 v8, a2\n"
      "v_mul_f32 v8, v8, v7\n"
      "v_accvgpr_write_b32 a2, v8\n"
      "v_accvgpr_read_b32 v9, a3\n"
      "v_mul_f32 v9, v9, v7\n"
      "v_accvgpr_write_b32 a3, v9\n"
      "v_accvgpr_read_b32 v8, a4\n"
      "v_mul_f32 v8, v8, v7\n"
      "v_accvgpr_write_b32 a4, v8\n"
      "v_accvgpr_read_b32 v9, a5\n"
      "v_mul_f32 v9, v9, v7\n"
      "v_accvgpr_write_b32 a5, v9\n"
      "v_accvgpr_read_b32 v8, a6\n"
      "v_mul_f32 v8, v8, v7\n"
      "v_accvgpr_write_b32 a6, v8\n"
      "v_accvgpr_read_b32 v9, a7\n"
      "v_mul_f32 v9, v9, v7\n"
      "v_accvgpr_write_b32 a7, v9\n"
      "v_accvgpr_read_b32 v8, a8\n"
      "v_mul_f32 v8, v8, v7\n"
      "v_accvgpr_write_b32 a8, v8\n"
      "v_accvgpr_read_b32 v9, a9\n"
      "v_mul_f32 v9, v9, v7\n"
      "v_accvgpr_write_b32 a9, v9\n"
      "v_accvgpr_read_b32 v8, a10\n"
      "v_mul_f32 v8, v8, v7\n"
      "v_accvgpr_write_b32 a10, v8\n"
      "v_accvgpr_read_b32 v9, a11\n"
      "v_mul_f32 v9, v9, v7\n"
      "v_accvgpr_write_b32 a11, v9\n"
      "v_accvgpr_read_b32 v8, a12\n"
      "v_mul_f32 v8, v8, v7\n"
      "v_accvgpr_write_b32 a12, v8\n"
      "v_accvgpr_read_b32 v9, a13\n"
      "v_mul_f32 v9, v9, v7\n"
      "v_accvgpr_write_b32 a13, v9\n"
      "v_accvgpr_read_b32 v8, a14\n"
      "v_mul_f32 v8, v8, v7\n"
      "v_accvgpr_write_b32 a14, v8\n"
      "v_accvgpr_read_b32 v9, a15\n"
      "v_mul_f32 v9, v9, v7\n"
      "v_accvgpr_write_b32 a15, v9\n"
      "v_accvgpr_read_b32 v8, a16\n"
      "v_mul_f32 v8, v8, v7\n"
      "v_accvgpr_write_b32 a16, v8\n"
      "v_accvgpr_read_b32 v9, a17\n"
      "v_mul_f32 v9, v9, v7\n"
      "v_accvgpr_write_b32 a17, v9\n"
      "v_accvgpr_read_b32 v8, a18\n"
      "v_mul_f32 v8, v8, v7\n"
      "v_accvgpr_write_b32 a18, v8\n"
      "v_accvgpr_read_b32 v9, a19\n"
      "v_mul_f32 v9, v9, v7\n"
      "v_accvgpr_write_b32 a19, v9\n"
      "v_accvgpr_read_b32 v8, a20\n"
      "v_mul_f32 v8, v8, v7\n"
      "v_accvgpr_write_b32 a20, v8\n"
      "v_accvgpr_read_b32 v9, a21\n"
      "v_mul_f32 v9, v9, v7\n"
      "v_accvgpr_write_b32 a21, v9\n"
      "v_accvgpr_read_b32 v8, a22\n"
      "v_mul_f32 v8, v8, v7\n"
      "v_accvgpr_write_b32 a22, v8\n"
      "v_accvgpr_read_b32 v9, a23\n"
      "v_mul_f32 v9, v9, v7\n"
      "v_accvgpr_write_b32 a23, v9\n"
      "v_accvgpr_read_b32 v8, a24\n"
      "v_mul_f32 v8, v8, v7\n"
      "v_accvgpr_write_b32 a24, v8\n"
      "v_accvgpr_read_b32 v9, a25\n"
      "v_mul_f32 v9, v9, v7\n"
      "v_accvgpr_write_b32 a25, v9\n"
      "v_accvgpr_read_b32 v8, a26\n"
      "v_mul_f32 v8, v8, v7\n"
      "v_accvgpr_write_b32 a26, v8\n"
      "v_accvgpr_read_b32 v9, a27\n"
      "v_mul_f32 v9, v9, v7\n"
      "v_accvgpr_write_b32 a27, v9\n"
      "v_accvgpr_read_b32 v8, a28\n"
      "v_mul_f32 v8, v8, v7\n"
      "v_accvgpr_write_b32 a28, v8\n"
      "v_accvgpr_read_b32 v9, a29\n"
      "v_mul_f32 v9, v9, v7\n"
      "v_accvgpr_write_b32 a29, v9\n"
      "v_accvgpr_read_b32 v8, a30\n"
      "v_mul_f32 v8, v8, v7\n"
      "v_accvgpr_write_b32 a30, v8\n"
      "v_accvgpr_read_b32 v9, a31\n"
      "v_mul_f32 v9, v9, v7\n"
      "v_accvgpr_write_b32 a31, v9\n"
      "v_accvgpr_read_b32 v8, a32\n"
      "v_mul_f32 v8, v8, v7\n"
      "v_accvgpr_write_b32 a32, v8\n"
      "v_accvgpr_read_b32 v9, a33\n"
      "v_mul_f32 v9, v9, v7\n"
      "v_accvgpr_write_b32 a33, v9\n"
      "v_accvgpr_read_b32 v8, a34\n"
      "v_mul_f32 v8, v8, v7\n"
      "v_accvgpr_write_b32 a34, v8\n"
      "v_accvgpr_read_b32 v9, a35\n"
      "v_mul_f32 v9, v9, v7\n"
      "v_accvgpr_write_b32 a35, v9\n"
      "v_accvgpr_read_b32 v8, a36\n"
      "v_mul_f32 v8, v8, v7\n"
      "v_accvgpr_write_b32 a36, v8\n"
      "v_accvgpr_read_b32 v9, a37\n"
      "v_mul_f32 v9, v9, v7\n"
      "v_accvgpr_write_b32 a37, v9\n"
      "v_accvgpr_read_b32 v8, a38\n"
      "v_mul_f32 v8, v8, v7\n"
      "v_accvgpr_write_b32 a38, v8\n"
      "v_accvgpr_read_b32 v9, a39\n"
      "v_mul_f32 v9, v9, v7\n"
      "v_accvgpr_write_b32 a39, v9\n"
      "v_accvgpr_read_b32 v8, a40\n"
      "v_mul_f32 v8, v8, v7\n"
      "v_accvgpr_write_b32 a40, v8\n"
      "v_accvgpr_read_b32 v9, a41\n"
      "v_mul_f32 v9, v9, v7\n"
      "v_accvgpr_write_b32 a41, v9\n"
      "v_accvgpr_read_b32 v8, a42\n"
      "v_mul_f32 v8, v8, v7\n"
      "v_accvgpr_write_b32 a42, v8\n"
      "v_accvgpr_read_b32 v9, a43\n"
      "v_mul_f32 v9, v9, v7\n"
      "v_accvgpr_write_b32 a43, v9\n"
      "v_accvgpr_read_b32 v8, a44\n"
      "v_mul_f32 v8, v8, v7\n"
      "v_accvgpr_write_b32 a44, v8\n"
      "v_accvgpr_read_b32 v9, a45\n"
      "v_mul_f32 v9, v9, v7\n"
      "v_accvgpr_write_b32 a45, v9\n"
      "v_accvgpr_read_b32 v8, a46\n"
      "v_mul_f32 v8, v8, v7\n"
      "v_accvgpr_write_b32 a46, v8\n"
      "v_accvgpr_read_b32 v9, a47\n"
      "v_mul_f32 v9, v9, v7\n"
      "v_accvgpr_write_b32 a47, v9\n"
      "v_accvgpr_read_b32 v8, a48\n"
      "v_mul_f32 v8, v8, v7\n"
      "v_accvgpr_write_b32 a48, v8\n"
      "v_accvgpr_read_b32 v9, a49\n"
      "v_mul_f32 v9, v9, v7\n"
      "v_accvgpr_write_b32 a49, v9\n"
      "v_accvgpr_read_b32 v8, a50\n"
      "v_mul_f32 v8, v8, v7\n"
      "v_accvgpr_write_b32 a50, v8\n"
      "v_accvgpr_read_b32 v9, a51\n"
      "v_mul_f32 v9, v9, v7\n"
      "v_accvgpr_write_b32 a51, v9\n"
      "v_accvgpr_read_b32 v8, a52\n"
      "v_mul_f32 v8, v8, v7\n"
      "v_accvgpr_write_b32 a52, v8\n"
      "v_accvgpr_read_b32 v9, a53\n"
      "v_mul_f32 v9, v9, v7\n"
      "v_accvgpr_write_b32 a53, v9\n"
      "v_accvgpr_read_b32 v8, a54\n"
      "v_mul_f32 v8, v8, v7\n"
      "v_accvgpr_write_b32 a54, v8\n"
      "v_accvgpr_read_b32 v9, a55\n"
      "v_mul_f32 v9, v9, v7\n"
      "v_accvgpr_write_b32 a55, v9\n"
      "v_accvgpr_read_b32 v8, a56\n"
      "v_mul_f32 v8, v8, v7\n"
      "v_accvgpr_write_b32 a56, v8\n"
      "v_accvgpr_read_b32 v9, a57\n"
      "v_mul_f32 v9, v9, v7\n"
      "v_accvgpr_write_b32 a57, v9\n"
      "v_accvgpr_read_b32 v8, a58\n"
      "v_mul_f32 v8, v8, v7\n"
      "v_accvgpr_write_b32 a58, v8\n"
      "v_accvgpr_read_b32 v9, a59\n"
      "v_mul_f32 v9, v9, v7\n"
      "v_accvgpr_write_b32 a59, v9\n"
      "v_accvgpr_read_b32 v8, a60\n"
      "v_mul_f32 v8, v8, v7\n"
      "v_accvgpr_write_b32 a60, v8\n"
      "v_accvgpr_read_b32 v9, a61\n"
      "v_mul_f32 v9, v9, v7\n"
      "v_accvgpr_write_b32 a61, v9\n"
      "v_accvgpr_read_b32 v8, a62\n"
      "v_mul_f32 v8, v8, v7\n"
      "v_accvgpr_write_b32 a62, v8\n"
      "v_accvgpr_read_b32 v9, a63\n"
      "v_mul_f32 v9, v9, v7\n"
      "v_accvgpr_write_b32 a63, v9\n"
      "v_accvgpr_read_b32 v8, a192\n"
      "v_mul_f32 v8, v8, v7\n"
      "v_accvgpr_write_b32 a192, v8\n"
      "v_accvgpr_read_b32 v9, a193\n"
      "v_mul_f32 v9, v9, v7\n"
      "v_accvgpr_write_b32 a193, v9\n"
      "v_accvgpr_read_b32 v8, a194\n"
      "v_mul_f32 v8, v8, v7\n"
      "v_accvgpr_write_b32 a194, v8\n"
      "v_accvgpr_read_b32 v9, a195\n"
      "v_mul_f32 v9, v9, v7\n"
      "v_accvgpr_write_b32 a195, v9\n"
      "v_max_f32 v6, v30, v32\n"
      "v_sub_f32 v42, v42, v6\n"
      "v_sub_f32 v43, v43, v6\n"
      "v_sub_f32 v44, v44, v6\n"
      "v_sub_f32 v45, v45, v6\n"
      "v_sub_f32 v170, v170, v6\n"
      "v_sub_f32 v171, v171, v6\n"
      "v_sub_f32 v172, v172, v6\n"
      "v_sub_f32 v173, v173, v6\n"
      "v_sub_f32 v174, v174, v6\n"
      "v_sub_f32 v175, v175, v6\n"
      "v_sub_f32 v176, v176, v6\n"
      "v_sub_f32 v177, v177, v6\n"
      "v_sub_f32 v178, v178, v6\n"
      "v_sub_f32 v179, v179, v6\n"
      "v_sub_f32 v180, v180, v6\n"
      "v_sub_f32 v181, v181, v6\n"
      "v_sub_f32 v182, v182, v6\n"
      "v_sub_f32 v183, v183, v6\n"
      "v_sub_f32 v184, v184, v6\n"
      "v_sub_f32 v185, v185, v6\n"
      "v_exp_f32 v7, -v6\n"
      "v_accvgpr_read_b32 v8, a64\n"
      "v_mul_f32 v8, v8, v7\n"
      "v_accvgpr_write_b32 a64, v8\n"
      "v_accvgpr_read_b32 v9, a65\n"
      "v_mul_f32 v9, v9, v7\n"
      "v_accvgpr_write_b32 a65, v9\n"
      "v_accvgpr_read_b32 v8, a66\n"
      "v_mul_f32 v8, v8, v7\n"
      "v_accvgpr_write_b32 a66, v8\n"
      "v_accvgpr_read_b32 v9, a67\n"
      "v_mul_f32 v9, v9, v7\n"
      "v_accvgpr_write_b32 a67, v9\n"
      "v_accvgpr_read_b32 v8, a68\n"
      "v_mul_f32 v8, v8, v7\n"
      "v_accvgpr_write_b32 a68, v8\n"
      "v_accvgpr_read_b32 v9, a69\n"
      "v_mul_f32 v9, v9, v7\n"
      "v_accvgpr_write_b32 a69, v9\n"
      "v_accvgpr_read_b32 v8, a70\n"
      "v_mul_f32 v8, v8, v7\n"
      "v_accvgpr_write_b32 a70, v8\n"
      "v_accvgpr_read_b32 v9, a71\n"
      "v_mul_f32 v9, v9, v7\n"
      "v_accvgpr_write_b32 a71, v9\n"
      "v_accvgpr_read_b32 v8, a72\n"
      "v_mul_f32 v8, v8, v7\n"
      "v_accvgpr_write_b32 a72, v8\n"
      "v_accvgpr_read_b32 v9, a73\n"
      "v_mul_f32 v9, v9, v7\n"
      "v_accvgpr_write_b32 a73, v9\n"
      "v_accvgpr_read_b32 v8, a74\n"
      "v_mul_f32 v8, v8, v7\n"
      "v_accvgpr_write_b32 a74, v8\n"
      "v_accvgpr_read_b32 v9, a75\n"
      "v_mul_f32 v9, v9, v7\n"
      "v_accvgpr_write_b32 a75, v9\n"
      "v_accvgpr_read_b32 v8, a76\n"
      "v_mul_f32 v8, v8, v7\n"
      "v_accvgpr_write_b32 a76, v8\n"
      "v_accvgpr_read_b32 v9, a77\n"
      "v_mul_f32 v9, v9, v7\n"
      "v_accvgpr_write_b32 a77, v9\n"
      "v_accvgpr_read_b32 v8, a78\n"
      "v_mul_f32 v8, v8, v7\n"
      "v_accvgpr_write_b32 a78, v8\n"
      "v_accvgpr_read_b32 v9, a79\n"
      "v_mul_f32 v9, v9, v7\n"
      "v_accvgpr_write_b32 a79, v9\n"
      "v_accvgpr_read_b32 v8, a80\n"
      "v_mul_f32 v8, v8, v7\n"
      "v_accvgpr_write_b32 a80, v8\n"
      "v_accvgpr_read_b32 v9, a81\n"
      "v_mul_f32 v9, v9, v7\n"
      "v_accvgpr_write_b32 a81, v9\n"
      "v_accvgpr_read_b32 v8, a82\n"
      "v_mul_f32 v8, v8, v7\n"
      "v_accvgpr_write_b32 a82, v8\n"
      "v_accvgpr_read_b32 v9, a83\n"
      "v_mul_f32 v9, v9, v7\n"
      "v_accvgpr_write_b32 a83, v9\n"
      "v_accvgpr_read_b32 v8, a84\n"
      "v_mul_f32 v8, v8, v7\n"
      "v_accvgpr_write_b32 a84, v8\n"
      "v_accvgpr_read_b32 v9, a85\n"
      "v_mul_f32 v9, v9, v7\n"
      "v_accvgpr_write_b32 a85, v9\n"
      "v_accvgpr_read_b32 v8, a86\n"
      "v_mul_f32 v8, v8, v7\n"
      "v_accvgpr_write_b32 a86, v8\n"
      "v_accvgpr_read_b32 v9, a87\n"
      "v_mul_f32 v9, v9, v7\n"
      "v_accvgpr_write_b32 a87, v9\n"
      "v_accvgpr_read_b32 v8, a88\n"
      "v_mul_f32 v8, v8, v7\n"
      "v_accvgpr_write_b32 a88, v8\n"
      "v_accvgpr_read_b32 v9, a89\n"
      "v_mul_f32 v9, v9, v7\n"
      "v_accvgpr_write_b32 a89, v9\n"
      "v_accvgpr_read_b32 v8, a90\n"
      "v_mul_f32 v8, v8, v7\n"
      "v_accvgpr_write_b32 a90, v8\n"
      "v_accvgpr_read_b32 v9, a91\n"
      "v_mul_f32 v9, v9, v7\n"
      "v_accvgpr_write_b32 a91, v9\n"
      "v_accvgpr_read_b32 v8, a92\n"
      "v_mul_f32 v8, v8, v7\n"
      "v_accvgpr_write_b32 a92, v8\n"
      "v_accvgpr_read_b32 v9, a93\n"
      "v_mul_f32 v9, v9, v7\n"
      "v_accvgpr_write_b32 a93, v9\n"
      "v_accvgpr_read_b32 v8, a94\n"
      "v_mul_f32 v8, v8, v7\n"
      "v_accvgpr_write_b32 a94, v8\n"
      "v_accvgpr_read_b32 v9, a95\n"
      "v_mul_f32 v9, v9, v7\n"
      "v_accvgpr_write_b32 a95, v9\n"
      "v_accvgpr_read_b32 v8, a96\n"
      "v_mul_f32 v8, v8, v7\n"
      "v_accvgpr_write_b32 a96, v8\n"
      "v_accvgpr_read_b32 v9, a97\n"
      "v_mul_f32 v9, v9, v7\n"
      "v_accvgpr_write_b32 a97, v9\n"
      "v_accvgpr_read_b32 v8, a98\n"
      "v_mul_f32 v8, v8, v7\n"
      "v_accvgpr_write_b32 a98, v8\n"
      "v_accvgpr_read_b32 v9, a99\n"
      "v_mul_f32 v9, v9, v7\n"
      "v_accvgpr_write_b32 a99, v9\n"
      "v_accvgpr_read_b32 v8, a100\n"
      "v_mul_f32 v8, v8, v7\n"
      "v_accvgpr_write_b32 a100, v8\n"
      "v_accvgpr_read_b32 v9, a101\n"
      "v_mul_f32 v9, v9, v7\n"
      "v_accvgpr_write_b32 a101, v9\n"
      "v_accvgpr_read_b32 v8, a102\n"
      "v_mul_f32 v8, v8, v7\n"
      "v_accvgpr_write_b32 a102, v8\n"
      "v_accvgpr_read_b32 v9, a103\n"
      "v_mul_f32 v9, v9, v7\n"
      "v_accvgpr_write_b32 a103, v9\n"
      "v_accvgpr_read_b32 v8, a104\n"
      "v_mul_f32 v8, v8, v7\n"
      "v_accvgpr_write_b32 a104, v8\n"
      "v_accvgpr_read_b32 v9, a105\n"
      "v_mul_f32 v9, v9, v7\n"
      "v_accvgpr_write_b32 a105, v9\n"
      "v_accvgpr_read_b32 v8, a106\n"
      "v_mul_f32 v8, v8, v7\n"
      "v_accvgpr_write_b32 a106, v8\n"
      "v_accvgpr_read_b32 v9, a107\n"
      "v_mul_f32 v9, v9, v7\n"
      "v_accvgpr_write_b32 a107, v9\n"
      "v_accvgpr_read_b32 v8, a108\n"
      "v_mul_f32 v8, v8, v7\n"
      "v_accvgpr_write_b32 a108, v8\n"
      "v_accvgpr_read_b32 v9, a109\n"
      "v_mul_f32 v9, v9, v7\n"
      "v_accvgpr_write_b32 a109, v9\n"
      "v_accvgpr_read_b32 v8, a110\n"
      "v_mul_f32 v8, v8, v7\n"
      "v_accvgpr_write_b32 a110, v8\n"
      "v_accvgpr_read_b32 v9, a111\n"
      "v_mul_f32 v9, v9, v7\n"
      "v_accvgpr_write_b32 a111, v9\n"
      "v_accvgpr_read_b32 v8, a112\n"
      "v_mul_f32 v8, v8, v7\n"
      "v_accvgpr_write_b32 a112, v8\n"
      "v_accvgpr_read_b32 v9, a113\n"
      "v_mul_f32 v9, v9, v7\n"
      "v_accvgpr_write_b32 a113, v9\n"
      "v_accvgpr_read_b32 v8, a114\n"
      "v_mul_f32 v8, v8, v7\n"
      "v_accvgpr_write_b32 a114, v8\n"
      "v_accvgpr_read_b32 v9, a115\n"
      "v_mul_f32 v9, v9, v7\n"
      "v_accvgpr_write_b32 a115, v9\n"
      "v_accvgpr_read_b32 v8, a116\n"
      "v_mul_f32 v8, v8, v7\n"
      "v_accvgpr_write_b32 a116, v8\n"
      "v_accvgpr_read_b32 v9, a117\n"
      "v_mul_f32 v9, v9, v7\n"
      "v_accvgpr_write_b32 a117, v9\n"
      "v_accvgpr_read_b32 v8, a118\n"
      "v_mul_f32 v8, v8, v7\n"
      "v_accvgpr_write_b32 a118, v8\n"
      "v_accvgpr_read_b32 v9, a119\n"
      "v_mul_f32 v9, v9, v7\n"
      "v_accvgpr_write_b32 a119, v9\n"
      "v_accvgpr_read_b32 v8, a120\n"
      "v_mul_f32 v8, v8, v7\n"
      "v_accvgpr_write_b32 a120, v8\n"
      "v_accvgpr_read_b32 v9, a121\n"
      "v_mul_f32 v9, v9, v7\n"
      "v_accvgpr_write_b32 a121, v9\n"
      "v_accvgpr_read_b32 v8, a122\n"
      "v_mul_f32 v8, v8, v7\n"
      "v_accvgpr_write_b32 a122, v8\n"
      "v_accvgpr_read_b32 v9, a123\n"
      "v_mul_f32 v9, v9, v7\n"
      "v_accvgpr_write_b32 a123, v9\n"
      "v_accvgpr_read_b32 v8, a124\n"
      "v_mul_f32 v8, v8, v7\n"
      "v_accvgpr_write_b32 a124, v8\n"
      "v_accvgpr_read_b32 v9, a125\n"
      "v_mul_f32 v9, v9, v7\n"
      "v_accvgpr_write_b32 a125, v9\n"
      "v_accvgpr_read_b32 v8, a126\n"
      "v_mul_f32 v8, v8, v7\n"
      "v_accvgpr_write_b32 a126, v8\n"
      "v_accvgpr_read_b32 v9, a127\n"
      "v_mul_f32 v9, v9, v7\n"
      "v_accvgpr_write_b32 a127, v9\n"
      "v_accvgpr_read_b32 v8, a196\n"
      "v_mul_f32 v8, v8, v7\n"
      "v_accvgpr_write_b32 a196, v8\n"
      "v_accvgpr_read_b32 v9, a197\n"
      "v_mul_f32 v9, v9, v7\n"
      "v_accvgpr_write_b32 a197, v9\n"
      "v_accvgpr_read_b32 v8, a198\n"
      "v_mul_f32 v8, v8, v7\n"
      "v_accvgpr_write_b32 a198, v8\n"
      "v_accvgpr_read_b32 v9, a199\n"
      "v_mul_f32 v9, v9, v7\n"
      "v_accvgpr_write_b32 a199, v9\n"
      "v_max_f32 v6, v31, v32\n"
      "v_sub_f32 v46, v46, v6\n"
      "v_sub_f32 v47, v47, v6\n"
      "v_sub_f32 v48, v48, v6\n"
      "v_sub_f32 v49, v49, v6\n"
      "v_sub_f32 v186, v186, v6\n"
      "v_sub_f32 v187, v187, v6\n"
      "v_sub_f32 v188, v188, v6\n"
      "v_sub_f32 v189, v189, v6\n"
      "v_sub_f32 v190, v190, v6\n"
      "v_sub_f32 v191, v191, v6\n"
      "v_sub_f32 v192, v192, v6\n"
      "v_sub_f32 v193, v193, v6\n"
      "v_sub_f32 v194, v194, v6\n"
      "v_sub_f32 v195, v195, v6\n"
      "v_sub_f32 v196, v196, v6\n"
      "v_sub_f32 v197, v197, v6\n"
      "v_sub_f32 v198, v198, v6\n"
      "v_sub_f32 v199, v199, v6\n"
      "v_sub_f32 v200, v200, v6\n"
      "v_sub_f32 v201, v201, v6\n"
      "v_exp_f32 v7, -v6\n"
      "v_accvgpr_read_b32 v8, a128\n"
      "v_mul_f32 v8, v8, v7\n"
      "v_accvgpr_write_b32 a128, v8\n"
      "v_accvgpr_read_b32 v9, a129\n"
      "v_mul_f32 v9, v9, v7\n"
      "v_accvgpr_write_b32 a129, v9\n"
      "v_accvgpr_read_b32 v8, a130\n"
      "v_mul_f32 v8, v8, v7\n"
      "v_accvgpr_write_b32 a130, v8\n"
      "v_accvgpr_read_b32 v9, a131\n"
      "v_mul_f32 v9, v9, v7\n"
      "v_accvgpr_write_b32 a131, v9\n"
      "v_accvgpr_read_b32 v8, a132\n"
      "v_mul_f32 v8, v8, v7\n"
      "v_accvgpr_write_b32 a132, v8\n"
      "v_accvgpr_read_b32 v9, a133\n"
      "v_mul_f32 v9, v9, v7\n"
      "v_accvgpr_write_b32 a133, v9\n"
      "v_accvgpr_read_b32 v8, a134\n"
      "v_mul_f32 v8, v8, v7\n"
      "v_accvgpr_write_b32 a134, v8\n"
      "v_accvgpr_read_b32 v9, a135\n"
      "v_mul_f32 v9, v9, v7\n"
      "v_accvgpr_write_b32 a135, v9\n"
      "v_accvgpr_read_b32 v8, a136\n"
      "v_mul_f32 v8, v8, v7\n"
      "v_accvgpr_write_b32 a136, v8\n"
      "v_accvgpr_read_b32 v9, a137\n"
      "v_mul_f32 v9, v9, v7\n"
      "v_accvgpr_write_b32 a137, v9\n"
      "v_accvgpr_read_b32 v8, a138\n"
      "v_mul_f32 v8, v8, v7\n"
      "v_accvgpr_write_b32 a138, v8\n"
      "v_accvgpr_read_b32 v9, a139\n"
      "v_mul_f32 v9, v9, v7\n"
      "v_accvgpr_write_b32 a139, v9\n"
      "v_accvgpr_read_b32 v8, a140\n"
      "v_mul_f32 v8, v8, v7\n"
      "v_accvgpr_write_b32 a140, v8\n"
      "v_accvgpr_read_b32 v9, a141\n"
      "v_mul_f32 v9, v9, v7\n"
      "v_accvgpr_write_b32 a141, v9\n"
      "v_accvgpr_read_b32 v8, a142\n"
      "v_mul_f32 v8, v8, v7\n"
      "v_accvgpr_write_b32 a142, v8\n"
      "v_accvgpr_read_b32 v9, a143\n"
      "v_mul_f32 v9, v9, v7\n"
      "v_accvgpr_write_b32 a143, v9\n"
      "v_accvgpr_read_b32 v8, a144\n"
      "v_mul_f32 v8, v8, v7\n"
      "v_accvgpr_write_b32 a144, v8\n"
      "v_accvgpr_read_b32 v9, a145\n"
      "v_mul_f32 v9, v9, v7\n"
      "v_accvgpr_write_b32 a145, v9\n"
      "v_accvgpr_read_b32 v8, a146\n"
      "v_mul_f32 v8, v8, v7\n"
      "v_accvgpr_write_b32 a146, v8\n"
      "v_accvgpr_read_b32 v9, a147\n"
      "v_mul_f32 v9, v9, v7\n"
      "v_accvgpr_write_b32 a147, v9\n"
      "v_accvgpr_read_b32 v8, a148\n"
      "v_mul_f32 v8, v8, v7\n"
      "v_accvgpr_write_b32 a148, v8\n"
      "v_accvgpr_read_b32 v9, a149\n"
      "v_mul_f32 v9, v9, v7\n"
      "v_accvgpr_write_b32 a149, v9\n"
      "v_accvgpr_read_b32 v8, a150\n"
      "v_mul_f32 v8, v8, v7\n"
      "v_accvgpr_write_b32 a150, v8\n"
      "v_accvgpr_read_b32 v9, a151\n"
      "v_mul_f32 v9, v9, v7\n"
      "v_accvgpr_write_b32 a151, v9\n"
      "v_accvgpr_read_b32 v8, a152\n"
      "v_mul_f32 v8, v8, v7\n"
      "v_accvgpr_write_b32 a152, v8\n"
      "v_accvgpr_read_b32 v9, a153\n"
      "v_mul_f32 v9, v9, v7\n"
      "v_accvgpr_write_b32 a153, v9\n"
      "v_accvgpr_read_b32 v8, a154\n"
      "v_mul_f32 v8, v8, v7\n"
      "v_accvgpr_write_b32 a154, v8\n"
      "v_accvgpr_read_b32 v9, a155\n"
      "v_mul_f32 v9, v9, v7\n"
      "v_accvgpr_write_b32 a155, v9\n"
      "v_accvgpr_read_b32 v8, a156\n"
      "v_mul_f32 v8, v8, v7\n"
      "v_accvgpr_write_b32 a156, v8\n"
      "v_accvgpr_read_b32 v9, a157\n"
      "v_mul_f32 v9, v9, v7\n"
      "v_accvgpr_write_b32 a157, v9\n"
      "v_accvgpr_read_b32 v8, a158\n"
      "v_mul_f32 v8, v8, v7\n"
      "v_accvgpr_write_b32 a158, v8\n"
      "v_accvgpr_read_b32 v9, a159\n"
      "v_mul_f32 v9, v9, v7\n"
      "v_accvgpr_write_b32 a159, v9\n"
      "v_accvgpr_read_b32 v8, a160\n"
      "v_mul_f32 v8, v8, v7\n"
      "v_accvgpr_write_b32 a160, v8\n"
      "v_accvgpr_read_b32 v9, a161\n"
      "v_mul_f32 v9, v9, v7\n"
      "v_accvgpr_write_b32 a161, v9\n"
      "v_accvgpr_read_b32 v8, a162\n"
      "v_mul_f32 v8, v8, v7\n"
      "v_accvgpr_write_b32 a162, v8\n"
      "v_accvgpr_read_b32 v9, a163\n"
      "v_mul_f32 v9, v9, v7\n"
      "v_accvgpr_write_b32 a163, v9\n"
      "v_accvgpr_read_b32 v8, a164\n"
      "v_mul_f32 v8, v8, v7\n"
      "v_accvgpr_write_b32 a164, v8\n"
      "v_accvgpr_read_b32 v9, a165\n"
      "v_mul_f32 v9, v9, v7\n"
      "v_accvgpr_write_b32 a165, v9\n"
      "v_accvgpr_read_b32 v8, a166\n"
      "v_mul_f32 v8, v8, v7\n"
      "v_accvgpr_write_b32 a166, v8\n"
      "v_accvgpr_read_b32 v9, a167\n"
      "v_mul_f32 v9, v9, v7\n"
      "v_accvgpr_write_b32 a167, v9\n"
      "v_accvgpr_read_b32 v8, a168\n"
      "v_mul_f32 v8, v8, v7\n"
      "v_accvgpr_write_b32 a168, v8\n"
      "v_accvgpr_read_b32 v9, a169\n"
      "v_mul_f32 v9, v9, v7\n"
      "v_accvgpr_write_b32 a169, v9\n"
      "v_accvgpr_read_b32 v8, a170\n"
      "v_mul_f32 v8, v8, v7\n"
      "v_accvgpr_write_b32 a170, v8\n"
      "v_accvgpr_read_b32 v9, a171\n"
      "v_mul_f32 v9, v9, v7\n"
      "v_accvgpr_write_b32 a171, v9\n"
      "v_accvgpr_read_b32 v8, a172\n"
      "v_mul_f32 v8, v8, v7\n"
      "v_accvgpr_write_b32 a172, v8\n"
      "v_accvgpr_read_b32 v9, a173\n"
      "v_mul_f32 v9, v9, v7\n"
      "v_accvgpr_write_b32 a173, v9\n"
      "v_accvgpr_read_b32 v8, a174\n"
      "v_mul_f32 v8, v8, v7\n"
      "v_accvgpr_write_b32 a174, v8\n"
      "v_accvgpr_read_b32 v9, a175\n"
      "v_mul_f32 v9, v9, v7\n"
      "v_accvgpr_write_b32 a175, v9\n"
      "v_accvgpr_read_b32 v8, a176\n"
      "v_mul_f32 v8, v8, v7\n"
      "v_accvgpr_write_b32 a176, v8\n"
      "v_accvgpr_read_b32 v9, a177\n"
      "v_mul_f32 v9, v9, v7\n"
      "v_accvgpr_write_b32 a177, v9\n"
      "v_accvgpr_read_b32 v8, a178\n"
      "v_mul_f32 v8, v8, v7\n"
      "v_accvgpr_write_b32 a178, v8\n"
      "v_accvgpr_read_b32 v9, a179\n"
      "v_mul_f32 v9, v9, v7\n"
      "v_accvgpr_write_b32 a179, v9\n"
      "v_accvgpr_read_b32 v8, a180\n"
      "v_mul_f32 v8, v8, v7\n"
      "v_accvgpr_write_b32 a180, v8\n"
      "v_accvgpr_read_b32 v9, a181\n"
      "v_mul_f32 v9, v9, v7\n"
      "v_accvgpr_write_b32 a181, v9\n"
      "v_accvgpr_read_b32 v8, a182\n"
      "v_mul_f32 v8, v8, v7\n"
      "v_accvgpr_write_b32 a182, v8\n"
      "v_accvgpr_read_b32 v9, a183\n"
      "v_mul_f32 v9, v9, v7\n"
      "v_accvgpr_write_b32 a183, v9\n"
      "v_accvgpr_read_b32 v8, a184\n"
      "v_mul_f32 v8, v8, v7\n"
      "v_accvgpr_write_b32 a184, v8\n"
      "v_accvgpr_read_b32 v9, a185\n"
      "v_mul_f32 v9, v9, v7\n"
      "v_accvgpr_write_b32 a185, v9\n"
      "v_accvgpr_read_b32 v8, a186\n"
      "v_mul_f32 v8, v8, v7\n"
      "v_accvgpr_write_b32 a186, v8\n"
      "v_accvgpr_read_b32 v9, a187\n"
      "v_mul_f32 v9, v9, v7\n"
      "v_accvgpr_write_b32 a187, v9\n"
      "v_accvgpr_read_b32 v8, a188\n"
      "v_mul_f32 v8, v8, v7\n"
      "v_accvgpr_write_b32 a188, v8\n"
      "v_accvgpr_read_b32 v9, a189\n"
      "v_mul_f32 v9, v9, v7\n"
      "v_accvgpr_write_b32 a189, v9\n"
      "v_accvgpr_read_b32 v8, a190\n"
      "v_mul_f32 v8, v8, v7\n"
      "v_accvgpr_write_b32 a190, v8\n"
      "v_accvgpr_read_b32 v9, a191\n"
      "v_mul_f32 v9, v9, v7\n"
      "v_accvgpr_write_b32 a191, v9\n"
      "v_accvgpr_read_b32 v8, a200\n"
      "v_mul_f32 v8, v8, v7\n"
      "v_accvgpr_write_b32 a200, v8\n"
      "v_accvgpr_read_b32 v9, a201\n"
      "v_mul_f32 v9, v9, v7\n"
      "v_accvgpr_write_b32 a201, v9\n"
      "v_accvgpr_read_b32 v8, a202\n"
      "v_mul_f32 v8, v8, v7\n"
      "v_accvgpr_write_b32 a202, v8\n"
      "v_accvgpr_read_b32 v9, a203\n"
      "v_mul_f32 v9, v9, v7\n"
      "v_accvgpr_write_b32 a203, v9\n"
      "v_mov_b32 v32, 0\n"
      "s_nop 3\n"
      "s_branch BACK1_%=\n"
      "SLOW2_%=:\n"
      "s_nop 15\n"
      "v_max_f32 v6, v29, v32\n"
      "v_sub_f32 v38, v38, v6\n"
      "v_sub_f32 v39, v39, v6\n"
      "v_sub_f32 v40, v40, v6\n"
      "v_sub_f32 v41, v41, v6\n"
      "v_sub_f32 v106, v106, v6\n"
      "v_sub_f32 v107, v107, v6\n"
      "v_sub_f32 v108, v108, v6\n"
      "v_sub_f32 v109, v109, v6\n"
      "v_sub_f32 v110, v110, v6\n"
      "v_sub_f32 v111, v111, v6\n"
      "v_sub_f32 v112, v112, v6\n"
      "v_sub_f32 v113, v113, v6\n"
      "v_sub_f32 v114, v114, v6\n"
      "v_sub_f32 v115, v115, v6\n"
      "v_sub_f32 v116, v116, v6\n"
      "v_sub_f32 v117, v117, v6\n"
      "v_sub_f32 v118, v118, v6\n"
      "v_sub_f32 v119, v119, v6\n"
      "v_sub_f32 v120, v120, v6\n"
      "v_sub_f32 v121, v121, v6\n"
      "v_exp_f32 v7, -v6\n"
      "v_accvgpr_read_b32 v8, a0\n"
      "v_mul_f32 v8, v8, v7\n"
      "v_accvgpr_write_b32 a0, v8\n"
      "v_accvgpr_read_b32 v9, a1\n"
      "v_mul_f32 v9, v9, v7\n"
      "v_accvgpr_write_b32 a1, v9\n"
      "v_accvgpr_read_b32 v8, a2\n"
      "v_mul_f32 v8, v8, v7\n"
      "v_accvgpr_write_b32 a2, v8\n"
      "v_accvgpr_read_b32 v9, a3\n"
      "v_mul_f32 v9, v9, v7\n"
      "v_accvgpr_write_b32 a3, v9\n"
      "v_accvgpr_read_b32 v8, a4\n"
      "v_mul_f32 v8, v8, v7\n"
      "v_accvgpr_write_b32 a4, v8\n"
      "v_accvgpr_read_b32 v9, a5\n"
      "v_mul_f32 v9, v9, v7\n"
      "v_accvgpr_write_b32 a5, v9\n"
      "v_accvgpr_read_b32 v8, a6\n"
      "v_mul_f32 v8, v8, v7\n"
      "v_accvgpr_write_b32 a6, v8\n"
      "v_accvgpr_read_b32 v9, a7\n"
      "v_mul_f32 v9, v9, v7\n"
      "v_accvgpr_write_b32 a7, v9\n"
      "v_accvgpr_read_b32 v8, a8\n"
      "v_mul_f32 v8, v8, v7\n"
      "v_accvgpr_write_b32 a8, v8\n"
      "v_accvgpr_read_b32 v9, a9\n"
      "v_mul_f32 v9, v9, v7\n"
      "v_accvgpr_write_b32 a9, v9\n"
      "v_accvgpr_read_b32 v8, a10\n"
      "v_mul_f32 v8, v8, v7\n"
      "v_accvgpr_write_b32 a10, v8\n"
      "v_accvgpr_read_b32 v9, a11\n"
      "v_mul_f32 v9, v9, v7\n"
      "v_accvgpr_write_b32 a11, v9\n"
      "v_accvgpr_read_b32 v8, a12\n"
      "v_mul_f32 v8, v8, v7\n"
      "v_accvgpr_write_b32 a12, v8\n"
      "v_accvgpr_read_b32 v9, a13\n"
      "v_mul_f32 v9, v9, v7\n"
      "v_accvgpr_write_b32 a13, v9\n"
      "v_accvgpr_read_b32 v8, a14\n"
      "v_mul_f32 v8, v8, v7\n"
      "v_accvgpr_write_b32 a14, v8\n"
      "v_accvgpr_read_b32 v9, a15\n"
      "v_mul_f32 v9, v9, v7\n"
      "v_accvgpr_write_b32 a15, v9\n"
      "v_accvgpr_read_b32 v8, a16\n"
      "v_mul_f32 v8, v8, v7\n"
      "v_accvgpr_write_b32 a16, v8\n"
      "v_accvgpr_read_b32 v9, a17\n"
      "v_mul_f32 v9, v9, v7\n"
      "v_accvgpr_write_b32 a17, v9\n"
      "v_accvgpr_read_b32 v8, a18\n"
      "v_mul_f32 v8, v8, v7\n"
      "v_accvgpr_write_b32 a18, v8\n"
      "v_accvgpr_read_b32 v9, a19\n"
      "v_mul_f32 v9, v9, v7\n"
      "v_accvgpr_write_b32 a19, v9\n"
      "v_accvgpr_read_b32 v8, a20\n"
      "v_mul_f32 v8, v8, v7\n"
      "v_accvgpr_write_b32 a20, v8\n"
      "v_accvgpr_read_b32 v9, a21\n"
      "v_mul_f32 v9, v9, v7\n"
      "v_accvgpr_write_b32 a21, v9\n"
      "v_accvgpr_read_b32 v8, a22\n"
      "v_mul_f32 v8, v8, v7\n"
      "v_accvgpr_write_b32 a22, v8\n"
      "v_accvgpr_read_b32 v9, a23\n"
      "v_mul_f32 v9, v9, v7\n"
      "v_accvgpr_write_b32 a23, v9\n"
      "v_accvgpr_read_b32 v8, a24\n"
      "v_mul_f32 v8, v8, v7\n"
      "v_accvgpr_write_b32 a24, v8\n"
      "v_accvgpr_read_b32 v9, a25\n"
      "v_mul_f32 v9, v9, v7\n"
      "v_accvgpr_write_b32 a25, v9\n"
      "v_accvgpr_read_b32 v8, a26\n"
      "v_mul_f32 v8, v8, v7\n"
      "v_accvgpr_write_b32 a26, v8\n"
      "v_accvgpr_read_b32 v9, a27\n"
      "v_mul_f32 v9, v9, v7\n"
      "v_accvgpr_write_b32 a27, v9\n"
      "v_accvgpr_read_b32 v8, a28\n"
      "v_mul_f32 v8, v8, v7\n"
      "v_accvgpr_write_b32 a28, v8\n"
      "v_accvgpr_read_b32 v9, a29\n"
      "v_mul_f32 v9, v9, v7\n"
      "v_accvgpr_write_b32 a29, v9\n"
      "v_accvgpr_read_b32 v8, a30\n"
      "v_mul_f32 v8, v8, v7\n"
      "v_accvgpr_write_b32 a30, v8\n"
      "v_accvgpr_read_b32 v9, a31\n"
      "v_mul_f32 v9, v9, v7\n"
      "v_accvgpr_write_b32 a31, v9\n"
      "v_accvgpr_read_b32 v8, a32\n"
      "v_mul_f32 v8, v8, v7\n"
      "v_accvgpr_write_b32 a32, v8\n"
      "v_accvgpr_read_b32 v9, a33\n"
      "v_mul_f32 v9, v9, v7\n"
      "v_accvgpr_write_b32 a33, v9\n"
      "v_accvgpr_read_b32 v8, a34\n"
      "v_mul_f32 v8, v8, v7\n"
      "v_accvgpr_write_b32 a34, v8\n"
      "v_accvgpr_read_b32 v9, a35\n"
      "v_mul_f32 v9, v9, v7\n"
      "v_accvgpr_write_b32 a35, v9\n"
      "v_accvgpr_read_b32 v8, a36\n"
      "v_mul_f32 v8, v8, v7\n"
      "v_accvgpr_write_b32 a36, v8\n"
      "v_accvgpr_read_b32 v9, a37\n"
      "v_mul_f32 v9, v9, v7\n"
      "v_accvgpr_write_b32 a37, v9\n"
      "v_accvgpr_read_b32 v8, a38\n"
      "v_mul_f32 v8, v8, v7\n"
      "v_accvgpr_write_b32 a38, v8\n"
      "v_accvgpr_read_b32 v9, a39\n"
      "v_mul_f32 v9, v9, v7\n"
      "v_accvgpr_write_b32 a39, v9\n"
      "v_accvgpr_read_b32 v8, a40\n"
      "v_mul_f32 v8, v8, v7\n"
      "v_accvgpr_write_b32 a40, v8\n"
      "v_accvgpr_read_b32 v9, a41\n"
      "v_mul_f32 v9, v9, v7\n"
      "v_accvgpr_write_b32 a41, v9\n"
      "v_accvgpr_read_b32 v8, a42\n"
      "v_mul_f32 v8, v8, v7\n"
      "v_accvgpr_write_b32 a42, v8\n"
      "v_accvgpr_read_b32 v9, a43\n"
      "v_mul_f32 v9, v9, v7\n"
      "v_accvgpr_write_b32 a43, v9\n"
      "v_accvgpr_read_b32 v8, a44\n"
      "v_mul_f32 v8, v8, v7\n"
      "v_accvgpr_write_b32 a44, v8\n"
      "v_accvgpr_read_b32 v9, a45\n"
      "v_mul_f32 v9, v9, v7\n"
      "v_accvgpr_write_b32 a45, v9\n"
      "v_accvgpr_read_b32 v8, a46\n"
      "v_mul_f32 v8, v8, v7\n"
      "v_accvgpr_write_b32 a46, v8\n"
      "v_accvgpr_read_b32 v9, a47\n"
      "v_mul_f32 v9, v9, v7\n"
      "v_accvgpr_write_b32 a47, v9\n"
      "v_accvgpr_read_b32 v8, a48\n"
      "v_mul_f32 v8, v8, v7\n"
      "v_accvgpr_write_b32 a48, v8\n"
      "v_accvgpr_read_b32 v9, a49\n"
      "v_mul_f32 v9, v9, v7\n"
      "v_accvgpr_write_b32 a49, v9\n"
      "v_accvgpr_read_b32 v8, a50\n"
      "v_mul_f32 v8, v8, v7\n"
      "v_accvgpr_write_b32 a50, v8\n"
      "v_accvgpr_read_b32 v9, a51\n"
      "v_mul_f32 v9, v9, v7\n"
      "v_accvgpr_write_b32 a51, v9\n"
      "v_accvgpr_read_b32 v8, a52\n"
      "v_mul_f32 v8, v8, v7\n"
      "v_accvgpr_write_b32 a52, v8\n"
      "v_accvgpr_read_b32 v9, a53\n"
      "v_mul_f32 v9, v9, v7\n"
      "v_accvgpr_write_b32 a53, v9\n"
      "v_accvgpr_read_b32 v8, a54\n"
      "v_mul_f32 v8, v8, v7\n"
      "v_accvgpr_write_b32 a54, v8\n"
      "v_accvgpr_read_b32 v9, a55\n"
      "v_mul_f32 v9, v9, v7\n"
      "v_accvgpr_write_b32 a55, v9\n"
      "v_accvgpr_read_b32 v8, a56\n"
      "v_mul_f32 v8, v8, v7\n"
      "v_accvgpr_write_b32 a56, v8\n"
      "v_accvgpr_read_b32 v9, a57\n"
      "v_mul_f32 v9, v9, v7\n"
      "v_accvgpr_write_b32 a57, v9\n"
      "v_accvgpr_read_b32 v8, a58\n"
      "v_mul_f32 v8, v8, v7\n"
      "v_accvgpr_write_b32 a58, v8\n"
      "v_accvgpr_read_b32 v9, a59\n"
      "v_mul_f32 v9, v9, v7\n"
      "v_accvgpr_write_b32 a59, v9\n"
      "v_accvgpr_read_b32 v8, a60\n"
      "v_mul_f32 v8, v8, v7\n"
      "v_accvgpr_write_b32 a60, v8\n"
      "v_accvgpr_read_b32 v9, a61\n"
      "v_mul_f32 v9, v9, v7\n"
      "v_accvgpr_write_b32 a61, v9\n"
      "v_accvgpr_read_b32 v8, a62\n"
      "v_mul_f32 v8, v8, v7\n"
      "v_accvgpr_write_b32 a62, v8\n"
      "v_accvgpr_read_b32 v9, a63\n"
      "v_mul_f32 v9, v9, v7\n"
      "v_accvgpr_write_b32 a63, v9\n"
      "v_accvgpr_read_b32 v8, a192\n"
      "v_mul_f32 v8, v8, v7\n"
      "v_accvgpr_write_b32 a192, v8\n"
      "v_accvgpr_read_b32 v9, a193\n"
      "v_mul_f32 v9, v9, v7\n"
      "v_accvgpr_write_b32 a193, v9\n"
      "v_accvgpr_read_b32 v8, a194\n"
      "v_mul_f32 v8, v8, v7\n"
      "v_accvgpr_write_b32 a194, v8\n"
      "v_accvgpr_read_b32 v9, a195\n"
      "v_mul_f32 v9, v9, v7\n"
      "v_accvgpr_write_b32 a195, v9\n"
      "v_max_f32 v6, v30, v32\n"
      "v_sub_f32 v42, v42, v6\n"
      "v_sub_f32 v43, v43, v6\n"
      "v_sub_f32 v44, v44, v6\n"
      "v_sub_f32 v45, v45, v6\n"
      "v_sub_f32 v122, v122, v6\n"
      "v_sub_f32 v123, v123, v6\n"
      "v_sub_f32 v124, v124, v6\n"
      "v_sub_f32 v125, v125, v6\n"
      "v_sub_f32 v126, v126, v6\n"
      "v_sub_f32 v127, v127, v6\n"
      "v_sub_f32 v128, v128, v6\n"
      "v_sub_f32 v129, v129, v6\n"
      "v_sub_f32 v130, v130, v6\n"
      "v_sub_f32 v131, v131, v6\n"
      "v_sub_f32 v132, v132, v6\n"
      "v_sub_f32 v133, v133, v6\n"
      "v_sub_f32 v134, v134, v6\n"
      "v_sub_f32 v135, v135, v6\n"
      "v_sub_f32 v136, v136, v6\n"
      "v_sub_f32 v137, v137, v6\n"
      "v_exp_f32 v7, -v6\n"
      "v_accvgpr_read_b32 v8, a64\n"
      "v_mul_f32 v8, v8, v7\n"
      "v_accvgpr_write_b32 a64, v8\n"
      "v_accvgpr_read_b32 v9, a65\n"
      "v_mul_f32 v9, v9, v7\n"
      "v_accvgpr_write_b32 a65, v9\n"
      "v_accvgpr_read_b32 v8, a66\n"
      "v_mul_f32 v8, v8, v7\n"
      "v_accvgpr_write_b32 a66, v8\n"
      "v_accvgpr_read_b32 v9, a67\n"
      "v_mul_f32 v9, v9, v7\n"
      "v_accvgpr_write_b32 a67, v9\n"
      "v_accvgpr_read_b32 v8, a68\n"
      "v_mul_f32 v8, v8, v7\n"
      "v_accvgpr_write_b32 a68, v8\n"
      "v_accvgpr_read_b32 v9, a69\n"
      "v_mul_f32 v9, v9, v7\n"
      "v_accvgpr_write_b32 a69, v9\n"
      "v_accvgpr_read_b32 v8, a70\n"
      "v_mul_f32 v8, v8, v7\n"
      "v_accvgpr_write_b32 a70, v8\n"
      "v_accvgpr_read_b32 v9, a71\n"
      "v_mul_f32 v9, v9, v7\n"
      "v_accvgpr_write_b32 a71, v9\n"
      "v_accvgpr_read_b32 v8, a72\n"
      "v_mul_f32 v8, v8, v7\n"
      "v_accvgpr_write_b32 a72, v8\n"
      "v_accvgpr_read_b32 v9, a73\n"
      "v_mul_f32 v9, v9, v7\n"
      "v_accvgpr_write_b32 a73, v9\n"
      "v_accvgpr_read_b32 v8, a74\n"
      "v_mul_f32 v8, v8, v7\n"
      "v_accvgpr_write_b32 a74, v8\n"
      "v_accvgpr_read_b32 v9, a75\n"
      "v_mul_f32 v9, v9, v7\n"
      "v_accvgpr_write_b32 a75, v9\n"
      "v_accvgpr_read_b32 v8, a76\n"
      "v_mul_f32 v8, v8, v7\n"
      "v_accvgpr_write_b32 a76, v8\n"
      "v_accvgpr_read_b32 v9, a77\n"
      "v_mul_f32 v9, v9, v7\n"
      "v_accvgpr_write_b32 a77, v9\n"
      "v_accvgpr_read_b32 v8, a78\n"
      "v_mul_f32 v8, v8, v7\n"
      "v_accvgpr_write_b32 a78, v8\n"
      "v_accvgpr_read_b32 v9, a79\n"
      "v_mul_f32 v9, v9, v7\n"
      "v_accvgpr_write_b32 a79, v9\n"
      "v_accvgpr_read_b32 v8, a80\n"
      "v_mul_f32 v8, v8, v7\n"
      "v_accvgpr_write_b32 a80, v8\n"
      "v_accvgpr_read_b32 v9, a81\n"
      "v_mul_f32 v9, v9, v7\n"
      "v_accvgpr_write_b32 a81, v9\n"
      "v_accvgpr_read_b32 v8, a82\n"
      "v_mul_f32 v8, v8, v7\n"
      "v_accvgpr_write_b32 a82, v8\n"
      "v_accvgpr_read_b32 v9, a83\n"
      "v_mul_f32 v9, v9, v7\n"
      "v_accvgpr_write_b32 a83, v9\n"
      "v_accvgpr_read_b32 v8, a84\n"
      "v_mul_f32 v8, v8, v7\n"
      "v_accvgpr_write_b32 a84, v8\n"
      "v_accvgpr_read_b32 v9, a85\n"
      "v_mul_f32 v9, v9, v7\n"
      "v_accvgpr_write_b32 a85, v9\n"
      "v_accvgpr_read_b32 v8, a86\n"
      "v_mul_f32 v8, v8, v7\n"
      "v_accvgpr_write_b32 a86, v8\n"
      "v_accvgpr_read_b32 v9, a87\n"
      "v_mul_f32 v9, v9, v7\n"
      "v_accvgpr_write_b32 a87, v9\n"
      "v_accvgpr_read_b32 v8, a88\n"
      "v_mul_f32 v8, v8, v7\n"
      "v_accvgpr_write_b32 a88, v8\n"
      "v_accvgpr_read_b32 v9, a89\n"
      "v_mul_f32 v9, v9, v7\n"
      "v_accvgpr_write_b32 a89, v9\n"
      "v_accvgpr_read_b32 v8, a90\n"
      "v_mul_f32 v8, v8, v7\n"
      "v_accvgpr_write_b32 a90, v8\n"
      "v_accvgpr_read_b32 v9, a91\n"
      "v_mul_f32 v9, v9, v7\n"
      "v_accvgpr_write_b32 a91, v9\n"
      "v_accvgpr_read_b32 v8, a92\n"
      "v_mul_f32 v8, v8, v7\n"
      "v_accvgpr_write_b32 a92, v8\n"
      "v_accvgpr_read_b32 v9, a93\n"
      "v_mul_f32 v9, v9, v7\n"
      "v_accvgpr_write_b32 a93, v9\n"
      "v_accvgpr_read_b32 v8, a94\n"
      "v_mul_f32 v8, v8, v7\n"
      "v_accvgpr_write_b32 a94, v8\n"
      "v_accvgpr_read_b32 v9, a95\n"
      "v_mul_f32 v9, v9, v7\n"
      "v_accvgpr_write_b32 a95, v9\n"
      "v_accvgpr_read_b32 v8, a96\n"
      "v_mul_f32 v8, v8, v7\n"
      "v_accvgpr_write_b32 a96, v8\n"
      "v_accvgpr_read_b32 v9, a97\n"
      "v_mul_f32 v9, v9, v7\n"
      "v_accvgpr_write_b32 a97, v9\n"
      "v_accvgpr_read_b32 v8, a98\n"
      "v_mul_f32 v8, v8, v7\n"
      "v_accvgpr_write_b32 a98, v8\n"
      "v_accvgpr_read_b32 v9, a99\n"
      "v_mul_f32 v9, v9, v7\n"
      "v_accvgpr_write_b32 a99, v9\n"
      "v_accvgpr_read_b32 v8, a100\n"
      "v_mul_f32 v8, v8, v7\n"
      "v_accvgpr_write_b32 a100, v8\n"
      "v_accvgpr_read_b32 v9, a101\n"
      "v_mul_f32 v9, v9, v7\n"
      "v_accvgpr_write_b32 a101, v9\n"
      "v_accvgpr_read_b32 v8, a102\n"
      "v_mul_f32 v8, v8, v7\n"
      "v_accvgpr_write_b32 a102, v8\n"
      "v_accvgpr_read_b32 v9, a103\n"
      "v_mul_f32 v9, v9, v7\n"
      "v_accvgpr_write_b32 a103, v9\n"
      "v_accvgpr_read_b32 v8, a104\n"
      "v_mul_f32 v8, v8, v7\n"
      "v_accvgpr_write_b32 a104, v8\n"
      "v_accvgpr_read_b32 v9, a105\n"
      "v_mul_f32 v9, v9, v7\n"
      "v_accvgpr_write_b32 a105, v9\n"
      "v_accvgpr_read_b32 v8, a106\n"
      "v_mul_f32 v8, v8, v7\n"
      "v_accvgpr_write_b32 a106, v8\n"
      "v_accvgpr_read_b32 v9, a107\n"
      "v_mul_f32 v9, v9, v7\n"
      "v_accvgpr_write_b32 a107, v9\n"
      "v_accvgpr_read_b32 v8, a108\n"
      "v_mul_f32 v8, v8, v7\n"
      "v_accvgpr_write_b32 a108, v8\n"
      "v_accvgpr_read_b32 v9, a109\n"
      "v_mul_f32 v9, v9, v7\n"
      "v_accvgpr_write_b32 a109, v9\n"
      "v_accvgpr_read_b32 v8, a110\n"
      "v_mul_f32 v8, v8, v7\n"
      "v_accvgpr_write_b32 a110, v8\n"
      "v_accvgpr_read_b32 v9, a111\n"
      "v_mul_f32 v9, v9, v7\n"
      "v_accvgpr_write_b32 a111, v9\n"
      "v_accvgpr_read_b32 v8, a112\n"
      "v_mul_f32 v8, v8, v7\n"
      "v_accvgpr_write_b32 a112, v8\n"
      "v_accvgpr_read_b32 v9, a113\n"
      "v_mul_f32 v9, v9, v7\n"
      "v_accvgpr_write_b32 a113, v9\n"
      "v_accvgpr_read_b32 v8, a114\n"
      "v_mul_f32 v8, v8, v7\n"
      "v_accvgpr_write_b32 a114, v8\n"
      "v_accvgpr_read_b32 v9, a115\n"
      "v_mul_f32 v9, v9, v7\n"
      "v_accvgpr_write_b32 a115, v9\n"
      "v_accvgpr_read_b32 v8, a116\n"
      "v_mul_f32 v8, v8, v7\n"
      "v_accvgpr_write_b32 a116, v8\n"
      "v_accvgpr_read_b32 v9, a117\n"
      "v_mul_f32 v9, v9, v7\n"
      "v_accvgpr_write_b32 a117, v9\n"
      "v_accvgpr_read_b32 v8, a118\n"
      "v_mul_f32 v8, v8, v7\n"
      "v_accvgpr_write_b32 a118, v8\n"
      "v_accvgpr_read_b32 v9, a119\n"
      "v_mul_f32 v9, v9, v7\n"
      "v_accvgpr_write_b32 a119, v9\n"
      "v_accvgpr_read_b32 v8, a120\n"
      "v_mul_f32 v8, v8, v7\n"
      "v_accvgpr_write_b32 a120, v8\n"
      "v_accvgpr_read_b32 v9, a121\n"
      "v_mul_f32 v9, v9, v7\n"
      "v_accvgpr_write_b32 a121, v9\n"
      "v_accvgpr_read_b32 v8, a122\n"
      "v_mul_f32 v8, v8, v7\n"
      "v_accvgpr_write_b32 a122, v8\n"
      "v_accvgpr_read_b32 v9, a123\n"
      "v_mul_f32 v9, v9, v7\n"
      "v_accvgpr_write_b32 a123, v9\n"
      "v_accvgpr_read_b32 v8, a124\n"
      "v_mul_f32 v8, v8, v7\n"
      "v_accvgpr_write_b32 a124, v8\n"
      "v_accvgpr_read_b32 v9, a125\n"
      "v_mul_f32 v9, v9, v7\n"
      "v_accvgpr_write_b32 a125, v9\n"
      "v_accvgpr_read_b32 v8, a126\n"
      "v_mul_f32 v8, v8, v7\n"
      "v_accvgpr_write_b32 a126, v8\n"
      "v_accvgpr_read_b32 v9, a127\n"
      "v_mul_f32 v9, v9, v7\n"
      "v_accvgpr_write_b32 a127, v9\n"
      "v_accvgpr_read_b32 v8, a196\n"
      "v_mul_f32 v8, v8, v7\n"
      "v_accvgpr_write_b32 a196, v8\n"
      "v_accvgpr_read_b32 v9, a197\n"
      "v_mul_f32 v9, v9, v7\n"
      "v_accvgpr_write_b32 a197, v9\n"
      "v_accvgpr_read_b32 v8, a198\n"
      "v_mul_f32 v8, v8, v7\n"
      "v_accvgpr_write_b32 a198, v8\n"
      "v_accvgpr_read_b32 v9, a199\n"
      "v_mul_f32 v9, v9, v7\n"
      "v_accvgpr_write_b32 a199, v9\n"
      "v_max_f32 v6, v31, v32\n"
      "v_sub_f32 v46, v46, v6\n"
      "v_sub_f32 v47, v47, v6\n"
      "v_sub_f32 v48, v48, v6\n"
      "v_sub_f32 v49, v49, v6\n"
      "v_sub_f32 v138, v138, v6\n"
      "v_sub_f32 v139, v139, v6\n"
      "v_sub_f32 v140, v140, v6\n"
      "v_sub_f32 v141, v141, v6\n"
      "v_sub_f32 v142, v142, v6\n"
      "v_sub_f32 v143, v143, v6\n"
      "v_sub_f32 v144, v144, v6\n"
      "v_sub_f32 v145, v145, v6\n"
      "v_sub_f32 v146, v146, v6\n"
      "v_sub_f32 v147, v147, v6\n"
      "v_sub_f32 v148, v148, v6\n"
      "v_sub_f32 v149, v149, v6\n"
      "v_sub_f32 v150, v150, v6\n"
      "v_sub_f32 v151, v151, v6\n"
      "v_sub_f32 v152, v152, v6\n"
      "v_sub_f32 v153, v153, v6\n"
      "v_exp_f32 v7, -v6\n"
      "v_accvgpr_read_b32 v8, a128\n"
      "v_mul_f32 v8, v8, v7\n"
      "v_accvgpr_write_b32 a128, v8\n"
      "v_accvgpr_read_b32 v9, a129\n"
      "v_mul_f32 v9, v9, v7\n"
      "v_accvgpr_write_b32 a129, v9\n"
      "v_accvgpr_read_b32 v8, a130\n"
      "v_mul_f32 v8, v8, v7\n"
      "v_accvgpr_write_b32 a130, v8\n"
      "v_accvgpr_read_b32 v9, a131\n"
      "v_mul_f32 v9, v9, v7\n"
      "v_accvgpr_write_b32 a131, v9\n"
      "v_accvgpr_read_b32 v8, a132\n"
      "v_mul_f32 v8, v8, v7\n"
      "v_accvgpr_write_b32 a132, v8\n"
      "v_accvgpr_read_b32 v9, a133\n"
      "v_mul_f32 v9, v9, v7\n"
      "v_accvgpr_write_b32 a133, v9\n"
      "v_accvgpr_read_b32 v8, a134\n"
      "v_mul_f32 v8, v8, v7\n"
      "v_accvgpr_write_b32 a134, v8\n"
      "v_accvgpr_read_b32 v9, a135\n"
      "v_mul_f32 v9, v9, v7\n"
      "v_accvgpr_write_b32 a135, v9\n"
      "v_accvgpr_read_b32 v8, a136\n"
      "v_mul_f32 v8, v8, v7\n"
      "v_accvgpr_write_b32 a136, v8\n"
      "v_accvgpr_read_b32 v9, a137\n"
      "v_mul_f32 v9, v9, v7\n"
      "v_accvgpr_write_b32 a137, v9\n"
      "v_accvgpr_read_b32 v8, a138\n"
      "v_mul_f32 v8, v8, v7\n"
      "v_accvgpr_write_b32 a138, v8\n"
      "v_accvgpr_read_b32 v9, a139\n"
      "v_mul_f32 v9, v9, v7\n"
      "v_accvgpr_write_b32 a139, v9\n"
      "v_accvgpr_read_b32 v8, a140\n"
      "v_mul_f32 v8, v8, v7\n"
      "v_accvgpr_write_b32 a140, v8\n"
      "v_accvgpr_read_b32 v9, a141\n"
      "v_mul_f32 v9, v9, v7\n"
      "v_accvgpr_write_b32 a141, v9\n"
      "v_accvgpr_read_b32 v8, a142\n"
      "v_mul_f32 v8, v8, v7\n"
      "v_accvgpr_write_b32 a142, v8\n"
      "v_accvgpr_read_b32 v9, a143\n"
      "v_mul_f32 v9, v9, v7\n"
      "v_accvgpr_write_b32 a143, v9\n"
      "v_accvgpr_read_b32 v8, a144\n"
      "v_mul_f32 v8, v8, v7\n"
      "v_accvgpr_write_b32 a144, v8\n"
      "v_accvgpr_read_b32 v9, a145\n"
      "v_mul_f32 v9, v9, v7\n"
      "v_accvgpr_write_b32 a145, v9\n"
      "v_accvgpr_read_b32 v8, a146\n"
      "v_mul_f32 v8, v8, v7\n"
      "v_accvgpr_write_b32 a146, v8\n"
      "v_accvgpr_read_b32 v9, a147\n"
      "v_mul_f32 v9, v9, v7\n"
      "v_accvgpr_write_b32 a147, v9\n"
      "v_accvgpr_read_b32 v8, a148\n"
      "v_mul_f32 v8, v8, v7\n"
      "v_accvgpr_write_b32 a148, v8\n"
      "v_accvgpr_read_b32 v9, a149\n"
      "v_mul_f32 v9, v9, v7\n"
      "v_accvgpr_write_b32 a149, v9\n"
      "v_accvgpr_read_b32 v8, a150\n"
      "v_mul_f32 v8, v8, v7\n"
      "v_accvgpr_write_b32 a150, v8\n"
      "v_accvgpr_read_b32 v9, a151\n"
      "v_mul_f32 v9, v9, v7\n"
      "v_accvgpr_write_b32 a151, v9\n"
      "v_accvgpr_read_b32 v8, a152\n"
      "v_mul_f32 v8, v8, v7\n"
      "v_accvgpr_write_b32 a152, v8\n"
      "v_accvgpr_read_b32 v9, a153\n"
      "v_mul_f32 v9, v9, v7\n"
      "v_accvgpr_write_b32 a153, v9\n"
      "v_accvgpr_read_b32 v8, a154\n"
      "v_mul_f32 v8, v8, v7\n"
      "v_accvgpr_write_b32 a154, v8\n"
      "v_accvgpr_read_b32 v9, a155\n"
      "v_mul_f32 v9, v9, v7\n"
      "v_accvgpr_write_b32 a155, v9\n"
      "v_accvgpr_read_b32 v8, a156\n"
      "v_mul_f32 v8, v8, v7\n"
      "v_accvgpr_write_b32 a156, v8\n"
      "v_accvgpr_read_b32 v9, a157\n"
      "v_mul_f32 v9, v9, v7\n"
      "v_accvgpr_write_b32 a157, v9\n"
      "v_accvgpr_read_b32 v8, a158\n"
      "v_mul_f32 v8, v8, v7\n"
      "v_accvgpr_write_b32 a158, v8\n"
      "v_accvgpr_read_b32 v9, a159\n"
      "v_mul_f32 v9, v9, v7\n"
      "v_accvgpr_write_b32 a159, v9\n"
      "v_accvgpr_read_b32 v8, a160\n"
      "v_mul_f32 v8, v8, v7\n"
      "v_accvgpr_write_b32 a160, v8\n"
      "v_accvgpr_read_b32 v9, a161\n"
      "v_mul_f32 v9, v9, v7\n"
      "v_accvgpr_write_b32 a161, v9\n"
      "v_accvgpr_read_b32 v8, a162\n"
      "v_mul_f32 v8, v8, v7\n"
      "v_accvgpr_write_b32 a162, v8\n"
      "v_accvgpr_read_b32 v9, a163\n"
      "v_mul_f32 v9, v9, v7\n"
      "v_accvgpr_write_b32 a163, v9\n"
      "v_accvgpr_read_b32 v8, a164\n"
      "v_mul_f32 v8, v8, v7\n"
      "v_accvgpr_write_b32 a164, v8\n"
      "v_accvgpr_read_b32 v9, a165\n"
      "v_mul_f32 v9, v9, v7\n"
      "v_accvgpr_write_b32 a165, v9\n"
      "v_accvgpr_read_b32 v8, a166\n"
      "v_mul_f32 v8, v8, v7\n"
      "v_accvgpr_write_b32 a166, v8\n"
      "v_accvgpr_read_b32 v9, a167\n"
      "v_mul_f32 v9, v9, v7\n"
      "v_accvgpr_write_b32 a167, v9\n"
      "v_accvgpr_read_b32 v8, a168\n"
      "v_mul_f32 v8, v8, v7\n"
      "v_accvgpr_write_b32 a168, v8\n"
      "v_accvgpr_read_b32 v9, a169\n"
      "v_mul_f32 v9, v9, v7\n"
      "v_accvgpr_write_b32 a169, v9\n"
      "v_accvgpr_read_b32 v8, a170\n"
      "v_mul_f32 v8, v8, v7\n"
      "v_accvgpr_write_b32 a170, v8\n"
      "v_accvgpr_read_b32 v9, a171\n"
      "v_mul_f32 v9, v9, v7\n"
      "v_accvgpr_write_b32 a171, v9\n"
      "v_accvgpr_read_b32 v8, a172\n"
      "v_mul_f32 v8, v8, v7\n"
      "v_accvgpr_write_b32 a172, v8\n"
      "v_accvgpr_read_b32 v9, a173\n"
      "v_mul_f32 v9, v9, v7\n"
      "v_accvgpr_write_b32 a173, v9\n"
      "v_accvgpr_read_b32 v8, a174\n"
      "v_mul_f32 v8, v8, v7\n"
      "v_accvgpr_write_b32 a174, v8\n"
      "v_accvgpr_read_b32 v9, a175\n"
      "v_mul_f32 v9, v9, v7\n"
      "v_accvgpr_write_b32 a175, v9\n"
      "v_accvgpr_read_b32 v8, a176\n"
      "v_mul_f32 v8, v8, v7\n"
      "v_accvgpr_write_b32 a176, v8\n"
      "v_accvgpr_read_b32 v9, a177\n"
      "v_mul_f32 v9, v9, v7\n"
      "v_accvgpr_write_b32 a177, v9\n"
      "v_accvgpr_read_b32 v8, a178\n"
      "v_mul_f32 v8, v8, v7\n"
      "v_accvgpr_write_b32 a178, v8\n"
      "v_accvgpr_read_b32 v9, a179\n"
      "v_mul_f32 v9, v9, v7\n"
      "v_accvgpr_write_b32 a179, v9\n"
      "v_accvgpr_read_b32 v8, a180\n"
      "v_mul_f32 v8, v8, v7\n"
      "v_accvgpr_write_b32 a180, v8\n"
      "v_accvgpr_read_b32 v9, a181\n"
      "v_mul_f32 v9, v9, v7\n"
      "v_accvgpr_write_b32 a181, v9\n"
      "v_accvgpr_read_b32 v8, a182\n"
      "v_mul_f32 v8, v8, v7\n"
      "v_accvgpr_write_b32 a182, v8\n"
      "v_accvgpr_read_b32 v9, a183\n"
      "v_mul_f32 v9, v9, v7\n"
      "v_accvgpr_write_b32 a183, v9\n"
      "v_accvgpr_read_b32 v8, a184\n"
      "v_mul_f32 v8, v8, v7\n"
      "v_accvgpr_write_b32 a184, v8\n"
      "v_accvgpr_read_b32 v9, a185\n"
      "v_mul_f32 v9, v9, v7\n"
      "v_accvgpr_write_b32 a185, v9\n"
      "v_accvgpr_read_b32 v8, a186\n"
      "v_mul_f32 v8, v8, v7\n"
      "v_accvgpr_write_b32 a186, v8\n"
      "v_accvgpr_read_b32 v9, a187\n"
      "v_mul_f32 v9, v9, v7\n"
      "v_accvgpr_write_b32 a187, v9\n"
      "v_accvgpr_read_b32 v8, a188\n"
      "v_mul_f32 v8, v8, v7\n"
      "v_accvgpr_write_b32 a188, v8\n"
      "v_accvgpr_read_b32 v9, a189\n"
      "v_mul_f32 v9, v9, v7\n"
      "v_accvgpr_write_b32 a189, v9\n"
      "v_accvgpr_read_b32 v8, a190\n"
      "v_mul_f32 v8, v8, v7\n"
      "v_accvgpr_write_b32 a190, v8\n"
      "v_accvgpr_read_b32 v9, a191\n"
      "v_mul_f32 v9, v9, v7\n"
      "v_accvgpr_write_b32 a191, v9\n"
      "v_accvgpr_read_b32 v8, a200\n"
      "v_mul_f32 v8, v8, v7\n"
      "v_accvgpr_write_b32 a200, v8\n"
      "v_accvgpr_read_b32 v9, a201\n"
      "v_mul_f32 v9, v9, v7\n"
      "v_accvgpr_write_b32 a201, v9\n"
      "v_accvgpr_read_b32 v8, a202\n"
      "v_mul_f32 v8, v8, v7\n"
      "v_accvgpr_write_b32 a202, v8\n"
      "v_accvgpr_read_b32 v9, a203\n"
      "v_mul_f32 v9, v9, v7\n"
      "v_accvgpr_write_b32 a203, v9\n"
      "v_mov_b32 v32, 0\n"
      "s_nop 3\n"
      "s_branch BACK2_%=\n"
      "SLOW3_%=:\n"
      "s_nop 15\n"
      "v_max_f32 v6, v29, v32\n"
      "v_sub_f32 v38, v38, v6\n"
      "v_sub_f32 v39, v39, v6\n"
      "v_sub_f32 v40, v40, v6\n"
      "v_sub_f32 v41, v41, v6\n"
      "v_sub_f32 v154, v154, v6\n"
      "v_sub_f32 v155, v155, v6\n"
      "v_sub_f32 v156, v156, v6\n"
      "v_sub_f32 v157, v157, v6\n"
      "v_sub_f32 v158, v158, v6\n"
      "v_sub_f32 v159, v159, v6\n"
      "v_sub_f32 v160, v160, v6\n"
      "v_sub_f32 v161, v161, v6\n"
      "v_sub_f32 v162, v162, v6\n"
      "v_sub_f32 v163, v163, v6\n"
      "v_sub_f32 v164, v164, v6\n"
      "v_sub_f32 v165, v165, v6\n"
      "v_sub_f32 v166, v166, v6\n"
      "v_sub_f32 v167, v167, v6\n"
      "v_sub_f32 v168, v168, v6\n"
      "v_sub_f32 v169, v169, v6\n"
      "v_exp_f32 v7, -v6\n"
      "v_accvgpr_read_b32 v8, a0\n"
      "v_mul_f32 v8, v8, v7\n"
      "v_accvgpr_write_b32 a0, v8\n"
      "v_accvgpr_read_b32 v9, a1\n"
      "v_mul_f32 v9, v9, v7\n"
      "v_accvgpr_write_b32 a1, v9\n"
      "v_accvgpr_read_b32 v8, a2\n"
      "v_mul_f32 v8, v8, v7\n"
      "v_accvgpr_write_b32 a2, v8\n"
      "v_accvgpr_read_b32 v9, a3\n"
      "v_mul_f32 v9, v9, v7\n"
      "v_accvgpr_write_b32 a3, v9\n"
      "v_accvgpr_read_b32 v8, a4\n"
      "v_mul_f32 v8, v8, v7\n"
      "v_accvgpr_write_b32 a4, v8\n"
      "v_accvgpr_read_b32 v9, a5\n"
      "v_mul_f32 v9, v9, v7\n"
      "v_accvgpr_write_b32 a5, v9\n"
      "v_accvgpr_read_b32 v8, a6\n"
      "v_mul_f32 v8, v8, v7\n"
      "v_accvgpr_write_b32 a6, v8\n"
      "v_accvgpr_read_b32 v9, a7\n"
      "v_mul_f32 v9, v9, v7\n"
      "v_accvgpr_write_b32 a7, v9\n"
      "v_accvgpr_read_b32 v8, a8\n"
      "v_mul_f32 v8, v8, v7\n"
      "v_accvgpr_write_b32 a8, v8\n"
      "v_accvgpr_read_b32 v9, a9\n"
      "v_mul_f32 v9, v9, v7\n"
      "v_accvgpr_write_b32 a9, v9\n"
      "v_accvgpr_read_b32 v8, a10\n"
      "v_mul_f32 v8, v8, v7\n"
      "v_accvgpr_write_b32 a10, v8\n"
      "v_accvgpr_read_b32 v9, a11\n"
      "v_mul_f32 v9, v9, v7\n"
      "v_accvgpr_write_b32 a11, v9\n"
      "v_accvgpr_read_b32 v8, a12\n"
      "v_mul_f32 v8, v8, v7\n"
      "v_accvgpr_write_b32 a12, v8\n"
      "v_accvgpr_read_b32 v9, a13\n"
      "v_mul_f32 v9, v9, v7\n"
      "v_accvgpr_write_b32 a13, v9\n"
      "v_accvgpr_read_b32 v8, a14\n"
      "v_mul_f32 v8, v8, v7\n"
      "v_accvgpr_write_b32 a14, v8\n"
      "v_accvgpr_read_b32 v9, a15\n"
      "v_mul_f32 v9, v9, v7\n"
      "v_accvgpr_write_b32 a15, v9\n"
      "v_accvgpr_read_b32 v8, a16\n"
      "v_mul_f32 v8, v8, v7\n"
      "v_accvgpr_write_b32 a16, v8\n"
      "v_accvgpr_read_b32 v9, a17\n"
      "v_mul_f32 v9, v9, v7\n"
      "v_accvgpr_write_b32 a17, v9\n"
      "v_accvgpr_read_b32 v8, a18\n"
      "v_mul_f32 v8, v8, v7\n"
      "v_accvgpr_write_b32 a18, v8\n"
      "v_accvgpr_read_b32 v9, a19\n"
      "v_mul_f32 v9, v9, v7\n"
      "v_accvgpr_write_b32 a19, v9\n"
      "v_accvgpr_read_b32 v8, a20\n"
      "v_mul_f32 v8, v8, v7\n"
      "v_accvgpr_write_b32 a20, v8\n"
      "v_accvgpr_read_b32 v9, a21\n"
      "v_mul_f32 v9, v9, v7\n"
      "v_accvgpr_write_b32 a21, v9\n"
      "v_accvgpr_read_b32 v8, a22\n"
      "v_mul_f32 v8, v8, v7\n"
      "v_accvgpr_write_b32 a22, v8\n"
      "v_accvgpr_read_b32 v9, a23\n"
      "v_mul_f32 v9, v9, v7\n"
      "v_accvgpr_write_b32 a23, v9\n"
      "v_accvgpr_read_b32 v8, a24\n"
      "v_mul_f32 v8, v8, v7\n"
      "v_accvgpr_write_b32 a24, v8\n"
      "v_accvgpr_read_b32 v9, a25\n"
      "v_mul_f32 v9, v9, v7\n"
      "v_accvgpr_write_b32 a25, v9\n"
      "v_accvgpr_read_b32 v8, a26\n"
      "v_mul_f32 v8, v8, v7\n"
      "v_accvgpr_write_b32 a26, v8\n"
      "v_accvgpr_read_b32 v9, a27\n"
      "v_mul_f32 v9, v9, v7\n"
      "v_accvgpr_write_b32 a27, v9\n"
      "v_accvgpr_read_b32 v8, a28\n"
      "v_mul_f32 v8, v8, v7\n"
      "v_accvgpr_write_b32 a28, v8\n"
      "v_accvgpr_read_b32 v9, a29\n"
      "v_mul_f32 v9, v9, v7\n"
      "v_accvgpr_write_b32 a29, v9\n"
      "v_accvgpr_read_b32 v8, a30\n"
      "v_mul_f32 v8, v8, v7\n"
      "v_accvgpr_write_b32 a30, v8\n"
      "v_accvgpr_read_b32 v9, a31\n"
      "v_mul_f32 v9, v9, v7\n"
      "v_accvgpr_write_b32 a31, v9\n"
      "v_accvgpr_read_b32 v8, a32\n"
      "v_mul_f32 v8, v8, v7\n"
      "v_accvgpr_write_b32 a32, v8\n"
      "v_accvgpr_read_b32 v9, a33\n"
      "v_mul_f32 v9, v9, v7\n"
      "v_accvgpr_write_b32 a33, v9\n"
      "v_accvgpr_read_b32 v8, a34\n"
      "v_mul_f32 v8, v8, v7\n"
      "v_accvgpr_write_b32 a34, v8\n"
      "v_accvgpr_read_b32 v9, a35\n"
      "v_mul_f32 v9, v9, v7\n"
      "v_accvgpr_write_b32 a35, v9\n"
      "v_accvgpr_read_b32 v8, a36\n"
      "v_mul_f32 v8, v8, v7\n"
      "v_accvgpr_write_b32 a36, v8\n"
      "v_accvgpr_read_b32 v9, a37\n"
      "v_mul_f32 v9, v9, v7\n"
      "v_accvgpr_write_b32 a37, v9\n"
      "v_accvgpr_read_b32 v8, a38\n"
      "v_mul_f32 v8, v8, v7\n"
      "v_accvgpr_write_b32 a38, v8\n"
      "v_accvgpr_read_b32 v9, a39\n"
      "v_mul_f32 v9, v9, v7\n"
      "v_accvgpr_write_b32 a39, v9\n"
      "v_accvgpr_read_b32 v8, a40\n"
      "v_mul_f32 v8, v8, v7\n"
      "v_accvgpr_write_b32 a40, v8\n"
      "v_accvgpr_read_b32 v9, a41\n"
      "v_mul_f32 v9, v9, v7\n"
      "v_accvgpr_write_b32 a41, v9\n"
      "v_accvgpr_read_b32 v8, a42\n"
      "v_mul_f32 v8, v8, v7\n"
      "v_accvgpr_write_b32 a42, v8\n"
      "v_accvgpr_read_b32 v9, a43\n"
      "v_mul_f32 v9, v9, v7\n"
      "v_accvgpr_write_b32 a43, v9\n"
      "v_accvgpr_read_b32 v8, a44\n"
      "v_mul_f32 v8, v8, v7\n"
      "v_accvgpr_write_b32 a44, v8\n"
      "v_accvgpr_read_b32 v9, a45\n"
      "v_mul_f32 v9, v9, v7\n"
      "v_accvgpr_write_b32 a45, v9\n"
      "v_accvgpr_read_b32 v8, a46\n"
      "v_mul_f32 v8, v8, v7\n"
      "v_accvgpr_write_b32 a46, v8\n"
      "v_accvgpr_read_b32 v9, a47\n"
      "v_mul_f32 v9, v9, v7\n"
      "v_accvgpr_write_b32 a47, v9\n"
      "v_accvgpr_read_b32 v8, a48\n"
      "v_mul_f32 v8, v8, v7\n"
      "v_accvgpr_write_b32 a48, v8\n"
      "v_accvgpr_read_b32 v9, a49\n"
      "v_mul_f32 v9, v9, v7\n"
      "v_accvgpr_write_b32 a49, v9\n"
      "v_accvgpr_read_b32 v8, a50\n"
      "v_mul_f32 v8, v8, v7\n"
      "v_accvgpr_write_b32 a50, v8\n"
      "v_accvgpr_read_b32 v9, a51\n"
      "v_mul_f32 v9, v9, v7\n"
      "v_accvgpr_write_b32 a51, v9\n"
      "v_accvgpr_read_b32 v8, a52\n"
      "v_mul_f32 v8, v8, v7\n"
      "v_accvgpr_write_b32 a52, v8\n"
      "v_accvgpr_read_b32 v9, a53\n"
      "v_mul_f32 v9, v9, v7\n"
      "v_accvgpr_write_b32 a53, v9\n"
      "v_accvgpr_read_b32 v8, a54\n"
      "v_mul_f32 v8, v8, v7\n"
      "v_accvgpr_write_b32 a54, v8\n"
      "v_accvgpr_read_b32 v9, a55\n"
      "v_mul_f32 v9, v9, v7\n"
      "v_accvgpr_write_b32 a55, v9\n"
      "v_accvgpr_read_b32 v8, a56\n"
      "v_mul_f32 v8, v8, v7\n"
      "v_accvgpr_write_b32 a56, v8\n"
      "v_accvgpr_read_b32 v9, a57\n"
      "v_mul_f32 v9, v9, v7\n"
      "v_accvgpr_write_b32 a57, v9\n"
      "v_accvgpr_read_b32 v8, a58\n"
      "v_mul_f32 v8, v8, v7\n"
      "v_accvgpr_write_b32 a58, v8\n"
      "v_accvgpr_read_b32 v9, a59\n"
      "v_mul_f32 v9, v9, v7\n"
      "v_accvgpr_write_b32 a59, v9\n"
      "v_accvgpr_read_b32 v8, a60\n"
      "v_mul_f32 v8, v8, v7\n"
      "v_accvgpr_write_b32 a60, v8\n"
      "v_accvgpr_read_b32 v9, a61\n"
      "v_mul_f32 v9, v9, v7\n"
      "v_accvgpr_write_b32 a61, v9\n"
      "v_accvgpr_read_b32 v8, a62\n"
      "v_mul_f32 v8, v8, v7\n"
      "v_accvgpr_write_b32 a62, v8\n"
      "v_accvgpr_read_b32 v9, a63\n"
      "v_mul_f32 v9, v9, v7\n"
      "v_accvgpr_write_b32 a63, v9\n"
      "v_accvgpr_read_b32 v8, a192\n"
      "v_mul_f32 v8, v8, v7\n"
      "v_accvgpr_write_b32 a192, v8\n"
      "v_accvgpr_read_b32 v9, a193\n"
      "v_mul_f32 v9, v9, v7\n"
      "v_accvgpr_write_b32 a193, v9\n"
      "v_accvgpr_read_b32 v8, a194\n"
      "v_mul_f32 v8, v8, v7\n"
      "v_accvgpr_write_b32 a194, v8\n"
      "v_accvgpr_read_b32 v9, a195\n"
      "v_mul_f32 v9, v9, v7\n"
      "v_accvgpr_write_b32 a195, v9\n"
      "v_max_f32 v6, v30, v32\n"
      "v_sub_f32 v42, v42, v6\n"
      "v_sub_f32 v43, v43, v6\n"
      "v_sub_f32 v44, v44, v6\n"
      "v_sub_f32 v45, v45, v6\n"
      "v_sub_f32 v170, v170, v6\n"
      "v_sub_f32 v171, v171, v6\n"
      "v_sub_f32 v172, v172, v6\n"
      "v_sub_f32 v173, v173, v6\n"
      "v_sub_f32 v174, v174, v6\n"
      "v_sub_f32 v175, v175, v6\n"
      "v_sub_f32 v176, v176, v6\n"
      "v_sub_f32 v177, v177, v6\n"
      "v_sub_f32 v178, v178, v6\n"
      "v_sub_f32 v179, v179, v6\n"
      "v_sub_f32 v180, v180, v6\n"
      "v_sub_f32 v181, v181, v6\n"
      "v_sub_f32 v182, v182, v6\n"
      "v_sub_f32 v183, v183, v6\n"
      "v_sub_f32 v184, v184, v6\n"
      "v_sub_f32 v185, v185, v6\n"
      "v_exp_f32 v7, -v6\n"
      "v_accvgpr_read_b32 v8, a64\n"
      "v_mul_f32 v8, v8, v7\n"
      "v_accvgpr_write_b32 a64, v8\n"
      "v_accvgpr_read_b32 v9, a65\n"
      "v_mul_f32 v9, v9, v7\n"
      "v_accvgpr_write_b32 a65, v9\n"
      "v_accvgpr_read_b32 v8, a66\n"
      "v_mul_f32 v8, v8, v7\n"
      "v_accvgpr_write_b32 a66, v8\n"
      "v_accvgpr_read_b32 v9, a67\n"
      "v_mul_f32 v9, v9, v7\n"
      "v_accvgpr_write_b32 a67, v9\n"
      "v_accvgpr_read_b32 v8, a68\n"
      "v_mul_f32 v8, v8, v7\n"
      "v_accvgpr_write_b32 a68, v8\n"
      "v_accvgpr_read_b32 v9, a69\n"
      "v_mul_f32 v9, v9, v7\n"
      "v_accvgpr_write_b32 a69, v9\n"
      "v_accvgpr_read_b32 v8, a70\n"
      "v_mul_f32 v8, v8, v7\n"
      "v_accvgpr_write_b32 a70, v8\n"
      "v_accvgpr_read_b32 v9, a71\n"
      "v_mul_f32 v9, v9, v7\n"
      "v_accvgpr_write_b32 a71, v9\n"
      "v_accvgpr_read_b32 v8, a72\n"
      "v_mul_f32 v8, v8, v7\n"
      "v_accvgpr_write_b32 a72, v8\n"
      "v_accvgpr_read_b32 v9, a73\n"
      "v_mul_f32 v9, v9, v7\n"
      "v_accvgpr_write_b32 a73, v9\n"
      "v_accvgpr_read_b32 v8, a74\n"
      "v_mul_f32 v8, v8, v7\n"
      "v_accvgpr_write_b32 a74, v8\n"
      "v_accvgpr_read_b32 v9, a75\n"
      "v_mul_f32 v9, v9, v7\n"
      "v_accvgpr_write_b32 a75, v9\n"
      "v_accvgpr_read_b32 v8, a76\n"
      "v_mul_f32 v8, v8, v7\n"
      "v_accvgpr_write_b32 a76, v8\n"
      "v_accvgpr_read_b32 v9, a77\n"
      "v_mul_f32 v9, v9, v7\n"
      "v_accvgpr_write_b32 a77, v9\n"
      "v_accvgpr_read_b32 v8, a78\n"
      "v_mul_f32 v8, v8, v7\n"
      "v_accvgpr_write_b32 a78, v8\n"
      "v_accvgpr_read_b32 v9, a79\n"
      "v_mul_f32 v9, v9, v7\n"
      "v_accvgpr_write_b32 a79, v9\n"
      "v_accvgpr_read_b32 v8, a80\n"
      "v_mul_f32 v8, v8, v7\n"
      "v_accvgpr_write_b32 a80, v8\n"
      "v_accvgpr_read_b32 v9, a81\n"
      "v_mul_f32 v9, v9, v7\n"
      "v_accvgpr_write_b32 a81, v9\n"
      "v_accvgpr_read_b32 v8, a82\n"
      "v_mul_f32 v8, v8, v7\n"
      "v_accvgpr_write_b32 a82, v8\n"
      "v_accvgpr_read_b32 v9, a83\n"
      "v_mul_f32 v9, v9, v7\n"
      "v_accvgpr_write_b32 a83, v9\n"
      "v_accvgpr_read_b32 v8, a84\n"
      "v_mul_f32 v8, v8, v7\n"
      "v_accvgpr_write_b32 a84, v8\n"
      "v_accvgpr_read_b32 v9, a85\n"
      "v_mul_f32 v9, v9, v7\n"
      "v_accvgpr_write_b32 a85, v9\n"
      "v_accvgpr_read_b32 v8, a86\n"
      "v_mul_f32 v8, v8, v7\n"
      "v_accvgpr_write_b32 a86, v8\n"
      "v_accvgpr_read_b32 v9, a87\n"
      "v_mul_f32 v9, v9, v7\n"
      "v_accvgpr_write_b32 a87, v9\n"
      "v_accvgpr_read_b32 v8, a88\n"
      "v_mul_f32 v8, v8, v7\n"
      "v_accvgpr_write_b32 a88, v8\n"
      "v_accvgpr_read_b32 v9, a89\n"
      "v_mul_f32 v9, v9, v7\n"
      "v_accvgpr_write_b32 a89, v9\n"
      "v_accvgpr_read_b32 v8, a90\n"
      "v_mul_f32 v8, v8, v7\n"
      "v_accvgpr_write_b32 a90, v8\n"
      "v_accvgpr_read_b32 v9, a91\n"
      "v_mul_f32 v9, v9, v7\n"
      "v_accvgpr_write_b32 a91, v9\n"
      "v_accvgpr_read_b32 v8, a92\n"
      "v_mul_f32 v8, v8, v7\n"
      "v_accvgpr_write_b32 a92, v8\n"
      "v_accvgpr_read_b32 v9, a93\n"
      "v_mul_f32 v9, v9, v7\n"
      "v_accvgpr_write_b32 a93, v9\n"
      "v_accvgpr_read_b32 v8, a94\n"
      "v_mul_f32 v8, v8, v7\n"
      "v_accvgpr_write_b32 a94, v8\n"
      "v_accvgpr_read_b32 v9, a95\n"
      "v_mul_f32 v9, v9, v7\n"
      "v_accvgpr_write_b32 a95, v9\n"
      "v_accvgpr_read_b32 v8, a96\n"
      "v_mul_f32 v8, v8, v7\n"
      "v_accvgpr_write_b32 a96, v8\n"
      "v_accvgpr_read_b32 v9, a97\n"
      "v_mul_f32 v9, v9, v7\n"
      "v_accvgpr_write_b32 a97, v9\n"
      "v_accvgpr_read_b32 v8, a98\n"
      "v_mul_f32 v8, v8, v7\n"
      "v_accvgpr_write_b32 a98, v8\n"
      "v_accvgpr_read_b32 v9, a99\n"
      "v_mul_f32 v9, v9, v7\n"
      "v_accvgpr_write_b32 a99, v9\n"
      "v_accvgpr_read_b32 v8, a100\n"
      "v_mul_f32 v8, v8, v7\n"
      "v_accvgpr_write_b32 a100, v8\n"
      "v_accvgpr_read_b32 v9, a101\n"
      "v_mul_f32 v9, v9, v7\n"
      "v_accvgpr_write_b32 a101, v9\n"
      "v_accvgpr_read_b32 v8, a102\n"
      "v_mul_f32 v8, v8, v7\n"
      "v_accvgpr_write_b32 a102, v8\n"
      "v_accvgpr_read_b32 v9, a103\n"
      "v_mul_f32 v9, v9, v7\n"
      "v_accvgpr_write_b32 a103, v9\n"
      "v_accvgpr_read_b32 v8, a104\n"
      "v_mul_f32 v8, v8, v7\n"
      "v_accvgpr_write_b32 a104, v8\n"
      "v_accvgpr_read_b32 v9, a105\n"
      "v_mul_f32 v9, v9, v7\n"
      "v_accvgpr_write_b32 a105, v9\n"
      "v_accvgpr_read_b32 v8, a106\n"
      "v_mul_f32 v8, v8, v7\n"
      "v_accvgpr_write_b32 a106, v8\n"
      "v_accvgpr_read_b32 v9, a107\n"
      "v_mul_f32 v9, v9, v7\n"
      "v_accvgpr_write_b32 a107, v9\n"
      "v_accvgpr_read_b32 v8, a108\n"
      "v_mul_f32 v8, v8, v7\n"
      "v_accvgpr_write_b32 a108, v8\n"
      "v_accvgpr_read_b32 v9, a109\n"
      "v_mul_f32 v9, v9, v7\n"
      "v_accvgpr_write_b32 a109, v9\n"
      "v_accvgpr_read_b32 v8, a110\n"
      "v_mul_f32 v8, v8, v7\n"
      "v_accvgpr_write_b32 a110, v8\n"
      "v_accvgpr_read_b32 v9, a111\n"
      "v_mul_f32 v9, v9, v7\n"
      "v_accvgpr_write_b32 a111, v9\n"
      "v_accvgpr_read_b32 v8, a112\n"
      "v_mul_f32 v8, v8, v7\n"
      "v_accvgpr_write_b32 a112, v8\n"
      "v_accvgpr_read_b32 v9, a113\n"
      "v_mul_f32 v9, v9, v7\n"
      "v_accvgpr_write_b32 a113, v9\n"
      "v_accvgpr_read_b32 v8, a114\n"
      "v_mul_f32 v8, v8, v7\n"
      "v_accvgpr_write_b32 a114, v8\n"
      "v_accvgpr_read_b32 v9, a115\n"
      "v_mul_f32 v9, v9, v7\n"
      "v_accvgpr_write_b32 a115, v9\n"
      "v_accvgpr_read_b32 v8, a116\n"
      "v_mul_f32 v8, v8, v7\n"
      "v_accvgpr_write_b32 a116, v8\n"
      "v_accvgpr_read_b32 v9, a117\n"
      "v_mul_f32 v9, v9, v7\n"
      "v_accvgpr_write_b32 a117, v9\n"
      "v_accvgpr_read_b32 v8, a118\n"
      "v_mul_f32 v8, v8, v7\n"
      "v_accvgpr_write_b32 a118, v8\n"
      "v_accvgpr_read_b32 v9, a119\n"
      "v_mul_f32 v9, v9, v7\n"
      "v_accvgpr_write_b32 a119, v9\n"
      "v_accvgpr_read_b32 v8, a120\n"
      "v_mul_f32 v8, v8, v7\n"
      "v_accvgpr_write_b32 a120, v8\n"
      "v_accvgpr_read_b32 v9, a121\n"
      "v_mul_f32 v9, v9, v7\n"
      "v_accvgpr_write_b32 a121, v9\n"
      "v_accvgpr_read_b32 v8, a122\n"
      "v_mul_f32 v8, v8, v7\n"
      "v_accvgpr_write_b32 a122, v8\n"
      "v_accvgpr_read_b32 v9, a123\n"
      "v_mul_f32 v9, v9, v7\n"
      "v_accvgpr_write_b32 a123, v9\n"
      "v_accvgpr_read_b32 v8, a124\n"
      "v_mul_f32 v8, v8, v7\n"
      "v_accvgpr_write_b32 a124, v8\n"
      "v_accvgpr_read_b32 v9, a125\n"
      "v_mul_f32 v9, v9, v7\n"
      "v_accvgpr_write_b32 a125, v9\n"
      "v_accvgpr_read_b32 v8, a126\n"
      "v_mul_f32 v8, v8, v7\n"
      "v_accvgpr_write_b32 a126, v8\n"
      "v_accvgpr_read_b32 v9, a127\n"
      "v_mul_f32 v9, v9, v7\n"
      "v_accvgpr_write_b32 a127, v9\n"
      "v_accvgpr_read_b32 v8, a196\n"
      "v_mul_f32 v8, v8, v7\n"
      "v_accvgpr_write_b32 a196, v8\n"
      "v_accvgpr_read_b32 v9, a197\n"
      "v_mul_f32 v9, v9, v7\n"
      "v_accvgpr_write_b32 a197, v9\n"
      "v_accvgpr_read_b32 v8, a198\n"
      "v_mul_f32 v8, v8, v7\n"
      "v_accvgpr_write_b32 a198, v8\n"
      "v_accvgpr_read_b32 v9, a199\n"
      "v_mul_f32 v9, v9, v7\n"
      "v_accvgpr_write_b32 a199, v9\n"
      "v_max_f32 v6, v31, v32\n"
      "v_sub_f32 v46, v46, v6\n"
      "v_sub_f32 v47, v47, v6\n"
      "v_sub_f32 v48, v48, v6\n"
      "v_sub_f32 v49, v49, v6\n"
      "v_sub_f32 v186, v186, v6\n"
      "v_sub_f32 v187, v187, v6\n"
      "v_sub_f32 v188, v188, v6\n"
      "v_sub_f32 v189, v189, v6\n"
      "v_sub_f32 v190, v190, v6\n"
      "v_sub_f32 v191, v191, v6\n"
      "v_sub_f32 v192, v192, v6\n"
      "v_sub_f32 v193, v193, v6\n"
      "v_sub_f32 v194, v194, v6\n"
      "v_sub_f32 v195, v195, v6\n"
      "v_sub_f32 v196, v196, v6\n"
      "v_sub_f32 v197, v197, v6\n"
      "v_sub_f32 v198, v198, v6\n"
      "v_sub_f32 v199, v199, v6\n"
      "v_sub_f32 v200, v200, v6\n"
      "v_sub_f32 v201, v201, v6\n"
      "v_exp_f32 v7, -v6\n"
      "v_accvgpr_read_b32 v8, a128\n"
      "v_mul_f32 v8, v8, v7\n"
      "v_accvgpr_write_b32 a128, v8\n"
      "v_accvgpr_read_b32 v9, a129\n"
      "v_mul_f32 v9, v9, v7\n"
      "v_accvgpr_write_b32 a129, v9\n"
      "v_accvgpr_read_b32 v8, a130\n"
      "v_mul_f32 v8, v8, v7\n"
      "v_accvgpr_write_b32 a130, v8\n"
      "v_accvgpr_read_b32 v9, a131\n"
      "v_mul_f32 v9, v9, v7\n"
      "v_accvgpr_write_b32 a131, v9\n"
      "v_accvgpr_read_b32 v8, a132\n"
      "v_mul_f32 v8, v8, v7\n"
      "v_accvgpr_write_b32 a132, v8\n"
      "v_accvgpr_read_b32 v9, a133\n"
      "v_mul_f32 v9, v9, v7\n"
      "v_accvgpr_write_b32 a133, v9\n"
      "v_accvgpr_read_b32 v8, a134\n"
      "v_mul_f32 v8, v8, v7\n"
      "v_accvgpr_write_b32 a134, v8\n"
      "v_accvgpr_read_b32 v9, a135\n"
      "v_mul_f32 v9, v9, v7\n"
      "v_accvgpr_write_b32 a135, v9\n"
      "v_accvgpr_read_b32 v8, a136\n"
      "v_mul_f32 v8, v8, v7\n"
      "v_accvgpr_write_b32 a136, v8\n"
      "v_accvgpr_read_b32 v9, a137\n"
      "v_mul_f32 v9, v9, v7\n"
      "v_accvgpr_write_b32 a137, v9\n"
      "v_accvgpr_read_b32 v8, a138\n"
      "v_mul_f32 v8, v8, v7\n"
      "v_accvgpr_write_b32 a138, v8\n"
      "v_accvgpr_read_b32 v9, a139\n"
      "v_mul_f32 v9, v9, v7\n"
      "v_accvgpr_write_b32 a139, v9\n"
      "v_accvgpr_read_b32 v8, a140\n"
      "v_mul_f32 v8, v8, v7\n"
      "v_accvgpr_write_b32 a140, v8\n"
      "v_accvgpr_read_b32 v9, a141\n"
      "v_mul_f32 v9, v9, v7\n"
      "v_accvgpr_write_b32 a141, v9\n"
      "v_accvgpr_read_b32 v8, a142\n"
      "v_mul_f32 v8, v8, v7\n"
      "v_accvgpr_write_b32 a142, v8\n"
      "v_accvgpr_read_b32 v9, a143\n"
      "v_mul_f32 v9, v9, v7\n"
      "v_accvgpr_write_b32 a143, v9\n"
      "v_accvgpr_read_b32 v8, a144\n"
      "v_mul_f32 v8, v8, v7\n"
      "v_accvgpr_write_b32 a144, v8\n"
      "v_accvgpr_read_b32 v9, a145\n"
      "v_mul_f32 v9, v9, v7\n"
      "v_accvgpr_write_b32 a145, v9\n"
      "v_accvgpr_read_b32 v8, a146\n"
      "v_mul_f32 v8, v8, v7\n"
      "v_accvgpr_write_b32 a146, v8\n"
      "v_accvgpr_read_b32 v9, a147\n"
      "v_mul_f32 v9, v9, v7\n"
      "v_accvgpr_write_b32 a147, v9\n"
      "v_accvgpr_read_b32 v8, a148\n"
      "v_mul_f32 v8, v8, v7\n"
      "v_accvgpr_write_b32 a148, v8\n"
      "v_accvgpr_read_b32 v9, a149\n"
      "v_mul_f32 v9, v9, v7\n"
      "v_accvgpr_write_b32 a149, v9\n"
      "v_accvgpr_read_b32 v8, a150\n"
      "v_mul_f32 v8, v8, v7\n"
      "v_accvgpr_write_b32 a150, v8\n"
      "v_accvgpr_read_b32 v9, a151\n"
      "v_mul_f32 v9, v9, v7\n"
      "v_accvgpr_write_b32 a151, v9\n"
      "v_accvgpr_read_b32 v8, a152\n"
      "v_mul_f32 v8, v8, v7\n"
      "v_accvgpr_write_b32 a152, v8\n"
      "v_accvgpr_read_b32 v9, a153\n"
      "v_mul_f32 v9, v9, v7\n"
      "v_accvgpr_write_b32 a153, v9\n"
      "v_accvgpr_read_b32 v8, a154\n"
      "v_mul_f32 v8, v8, v7\n"
      "v_accvgpr_write_b32 a154, v8\n"
      "v_accvgpr_read_b32 v9, a155\n"
      "v_mul_f32 v9, v9, v7\n"
      "v_accvgpr_write_b32 a155, v9\n"
      "v_accvgpr_read_b32 v8, a156\n"
      "v_mul_f32 v8, v8, v7\n"
      "v_accvgpr_write_b32 a156, v8\n"
      "v_accvgpr_read_b32 v9, a157\n"
      "v_mul_f32 v9, v9, v7\n"
      "v_accvgpr_write_b32 a157, v9\n"
      "v_accvgpr_read_b32 v8, a158\n"
      "v_mul_f32 v8, v8, v7\n"
      "v_accvgpr_write_b32 a158, v8\n"
      "v_accvgpr_read_b32 v9, a159\n"
      "v_mul_f32 v9, v9, v7\n"
      "v_accvgpr_write_b32 a159, v9\n"
      "v_accvgpr_read_b32 v8, a160\n"
      "v_mul_f32 v8, v8, v7\n"
      "v_accvgpr_write_b32 a160, v8\n"
      "v_accvgpr_read_b32 v9, a161\n"
      "v_mul_f32 v9, v9, v7\n"
      "v_accvgpr_write_b32 a161, v9\n"
      "v_accvgpr_read_b32 v8, a162\n"
      "v_mul_f32 v8, v8, v7\n"
      "v_accvgpr_write_b32 a162, v8\n"
      "v_accvgpr_read_b32 v9, a163\n"
      "v_mul_f32 v9, v9, v7\n"
      "v_accvgpr_write_b32 a163, v9\n"
      "v_accvgpr_read_b32 v8, a164\n"
      "v_mul_f32 v8, v8, v7\n"
      "v_accvgpr_write_b32 a164, v8\n"
      "v_accvgpr_read_b32 v9, a165\n"
      "v_mul_f32 v9, v9, v7\n"
      "v_accvgpr_write_b32 a165, v9\n"
      "v_accvgpr_read_b32 v8, a166\n"
      "v_mul_f32 v8, v8, v7\n"
      "v_accvgpr_write_b32 a166, v8\n"
      "v_accvgpr_read_b32 v9, a167\n"
      "v_mul_f32 v9, v9, v7\n"
      "v_accvgpr_write_b32 a167, v9\n"
      "v_accvgpr_read_b32 v8, a168\n"
      "v_mul_f32 v8, v8, v7\n"
      "v_accvgpr_write_b32 a168, v8\n"
      "v_accvgpr_read_b32 v9, a169\n"
      "v_mul_f32 v9, v9, v7\n"
      "v_accvgpr_write_b32 a169, v9\n"
      "v_accvgpr_read_b32 v8, a170\n"
      "v_mul_f32 v8, v8, v7\n"
      "v_accvgpr_write_b32 a170, v8\n"
      "v_accvgpr_read_b32 v9, a171\n"
      "v_mul_f32 v9, v9, v7\n"
      "v_accvgpr_write_b32 a171, v9\n"
      "v_accvgpr_read_b32 v8, a172\n"
      "v_mul_f32 v8, v8, v7\n"
      "v_accvgpr_write_b32 a172, v8\n"
      "v_accvgpr_read_b32 v9, a173\n"
      "v_mul_f32 v9, v9, v7\n"
      "v_accvgpr_write_b32 a173, v9\n"
      "v_accvgpr_read_b32 v8, a174\n"
      "v_mul_f32 v8, v8, v7\n"
      "v_accvgpr_write_b32 a174, v8\n"
      "v_accvgpr_read_b32 v9, a175\n"
      "v_mul_f32 v9, v9, v7\n"
      "v_accvgpr_write_b32 a175, v9\n"
      "v_accvgpr_read_b32 v8, a176\n"
      "v_mul_f32 v8, v8, v7\n"
      "v_accvgpr_write_b32 a176, v8\n"
      "v_accvgpr_read_b32 v9, a177\n"
      "v_mul_f32 v9, v9, v7\n"
      "v_accvgpr_write_b32 a177, v9\n"
      "v_accvgpr_read_b32 v8, a178\n"
      "v_mul_f32 v8, v8, v7\n"
      "v_accvgpr_write_b32 a178, v8\n"
      "v_accvgpr_read_b32 v9, a179\n"
      "v_mul_f32 v9, v9, v7\n"
      "v_accvgpr_write_b32 a179, v9\n"
      "v_accvgpr_read_b32 v8, a180\n"
      "v_mul_f32 v8, v8, v7\n"
      "v_accvgpr_write_b32 a180, v8\n"
      "v_accvgpr_read_b32 v9, a181\n"
      "v_mul_f32 v9, v9, v7\n"
      "v_accvgpr_write_b32 a181, v9\n"
      "v_accvgpr_read_b32 v8, a182\n"
      "v_mul_f32 v8, v8, v7\n"
      "v_accvgpr_write_b32 a182, v8\n"
      "v_accvgpr_read_b32 v9, a183\n"
      "v_mul_f32 v9, v9, v7\n"
      "v_accvgpr_write_b32 a183, v9\n"
      "v_accvgpr_read_b32 v8, a184\n"
      "v_mul_f32 v8, v8, v7\n"
      "v_accvgpr_write_b32 a184, v8\n"
      "v_accvgpr_read_b32 v9, a185\n"
      "v_mul_f32 v9, v9, v7\n"
      "v_accvgpr_write_b32 a185, v9\n"
      "v_accvgpr_read_b32 v8, a186\n"
      "v_mul_f32 v8, v8, v7\n"
      "v_accvgpr_write_b32 a186, v8\n"
      "v_accvgpr_read_b32 v9, a187\n"
      "v_mul_f32 v9, v9, v7\n"
      "v_accvgpr_write_b32 a187, v9\n"
      "v_accvgpr_read_b32 v8, a188\n"
      "v_mul_f32 v8, v8, v7\n"
      "v_accvgpr_write_b32 a188, v8\n"
      "v_accvgpr_read_b32 v9, a189\n"
      "v_mul_f32 v9, v9, v7\n"
      "v_accvgpr_write_b32 a189, v9\n"
      "v_accvgpr_read_b32 v8, a190\n"
      "v_mul_f32 v8, v8, v7\n"
      "v_accvgpr_write_b32 a190, v8\n"
      "v_accvgpr_read_b32 v9, a191\n"
      "v_mul_f32 v9, v9, v7\n"
      "v_accvgpr_write_b32 a191, v9\n"
      "v_accvgpr_read_b32 v8, a200\n"
      "v_mul_f32 v8, v8, v7\n"
      "v_accvgpr_write_b32 a200, v8\n"
      "v_accvgpr_read_b32 v9, a201\n"
      "v_mul_f32 v9, v9, v7\n"
      "v_accvgpr_write_b32 a201, v9\n"
      "v_accvgpr_read_b32 v8, a202\n"
      "v_mul_f32 v8, v8, v7\n"
      "v_accvgpr_write_b32 a202, v8\n"
      "v_accvgpr_read_b32 v9, a203\n"
      "v_mul_f32 v9, v9, v7\n"
      "v_accvgpr_write_b32 a203, v9\n"
      "v_mov_b32 v32, 0\n"
      "s_nop 3\n"
      "s_branch BACK3_%=\n"
      "END_%=:\n"
      : "+s"(qp), "+s"(kp), "+s"(vp), "+s"(op), "+s"(w8)
      :
      : "memory", "vcc", "scc", "v0", "v1", "v2", "v3", "v4", "v5", "v6", "v7", "v8", "v9", "v10", "v11", "v12", "v13", "v14", "v15", "v16", "v17", "v18", "v19", "v20", "v21", "v22", "v23", "v24", "v25", "v26", "v27", "v28", "v29", "v30", "v31", "v32", "v33", "v34", "v35", "v36", "v37", "v38", "v39", "v40", "v41", "v42", "v43", "v44", "v45", "v46", "v47", "v48", "v49", "v50", "v51", "v52", "v53", "v54", "v55", "v56", "v57", "v58", "v59", "v60", "v61", "v62", "v63", "v64", "v65", "v66", "v67", "v68", "v69", "v70", "v71", "v72", "v73", "v74", "v75", "v76", "v77", "v78", "v79", "v80", "v81", "v82", "v83", "v84", "v85", "v86", "v87", "v88", "v89", "v90", "v91", "v92", "v93", "v94", "v95", "v96", "v97", "v98", "v99", "v100", "v101", "v102", "v103", "v104", "v105", "v106", "v107", "v108", "v109", "v110", "v111", "v112", "v113", "v114", "v115", "v116", "v117", "v118", "v119", "v120", "v121", "v122", "v123", "v124", "v125", "v126", "v127", "v128", "v129", "v130", "v131", "v132", "v133", "v134", "v135", "v136", "v137", "v138", "v139", "v140", "v141", "v142", "v143", "v144", "v145", "v146", "v147", "v148", "v149", "v150", "v151", "v152", "v153", "v154", "v155", "v156", "v157", "v158", "v159", "v160", "v161", "v162", "v163", "v164", "v165", "v166", "v167", "v168", "v169", "v170", "v171", "v172", "v173", "v174", "v175", "v176", "v177", "v178", "v179", "v180", "v181", "v182", "v183", "v184", "v185", "v186", "v187", "v188", "v189", "v190", "v191", "v192", "v193", "v194", "v195", "v196", "v197", "v198", "v199", "v200", "v201", "v202", "v203", "v204", "v205", "v206", "v207", "v208", "v209", "v210", "v211", "v212", "v213", "v214", "v215", "v216", "v217", "v218", "v219", "v220", "v221", "v222", "v223", "v224", "v225", "v226", "v227", "v228", "v229", "v230", "v231", "v232", "v233", "v234", "v235", "v236", "v237", "v238", "v239", "v240", "v241", "v242", "v243", "v244", "v245", "v246", "v247", "v248", "v249", "v250", "v251", "v252", "v253", "v254", "v255", "a0", "a1", "a2", "a3", "a4", "a5", "a6", "a7", "a8", "a9", "a10", "a11", "a12", "a13", "a14", "a15", "a16", "a17", "a18", "a19", "a20", "a21", "a22", "a23", "a24", "a25", "a26", "a27", "a28", "a29", "a30", "a31", "a32", "a33", "a34", "a35", "a36", "a37", "a38", "a39", "a40", "a41", "a42", "a43", "a44", "a45", "a46", "a47", "a48", "a49", "a50", "a51", "a52", "a53", "a54", "a55", "a56", "a57", "a58", "a59", "a60", "a61", "a62", "a63", "a64", "a65", "a66", "a67", "a68", "a69", "a70", "a71", "a72", "a73", "a74", "a75", "a76", "a77", "a78", "a79", "a80", "a81", "a82", "a83", "a84", "a85", "a86", "a87", "a88", "a89", "a90", "a91", "a92", "a93", "a94", "a95", "a96", "a97", "a98", "a99", "a100", "a101", "a102", "a103", "a104", "a105", "a106", "a107", "a108", "a109", "a110", "a111", "a112", "a113", "a114", "a115", "a116", "a117", "a118", "a119", "a120", "a121", "a122", "a123", "a124", "a125", "a126", "a127", "a128", "a129", "a130", "a131", "a132", "a133", "a134", "a135", "a136", "a137", "a138", "a139", "a140", "a141", "a142", "a143", "a144", "a145", "a146", "a147", "a148", "a149", "a150", "a151", "a152", "a153", "a154", "a155", "a156", "a157", "a158", "a159", "a160", "a161", "a162", "a163", "a164", "a165", "a166", "a167", "a168", "a169", "a170", "a171", "a172", "a173", "a174", "a175", "a176", "a177", "a178", "a179", "a180", "a181", "a182", "a183", "a184", "a185", "a186", "a187", "a188", "a189", "a190", "a191", "a192", "a193", "a194", "a195", "a196", "a197", "a198", "a199", "a200", "a201", "a202", "a203", "a204", "a205", "a206", "a207", "a208", "a209", "a210", "a211", "a212", "a213", "a214", "a215", "a216", "a217", "a218", "a219", "a220", "a221", "a222", "a223", "a224", "a225", "a226", "a227", "a228", "a229", "a230", "a231", "a232", "a233", "a234", "a235", "a236", "a237", "a238", "a239", "a240", "a241", "a242", "a243", "a244", "a245", "a246", "a247", "a248", "a249", "a250", "a251", "a252", "a253", "a254", "a255", "s84", "s85", "s86", "s87", "s88", "s89", "s90", "s91", "s92", "s93", "s94", "s95");
}

extern "C" void kernel_launch(void* const* d_in, const int* in_sizes, int n_in, void* d_out, int out_size,
                              void* d_ws, size_t ws_size, hipStream_t stream) {
  mha_fwd<<<256, 256, 0, stream>>>((const __hip_bfloat16*)d_in[0], (const __hip_bfloat16*)d_in[1],
                                   (const __hip_bfloat16*)d_in[2], (__hip_bfloat16*)d_out);
}
```
